```python
import torch

def _softmax(x, axis=-1):
    return torch.softmax(x, dim=axis)
def _gelu(x):
    return torch.nn.functional.gelu(x, approximate="tanh")
def _silu(x):
    return torch.nn.functional.silu(x)
def _sigmoid(x):
    return torch.sigmoid(x)
def _layernorm(x, axis=-1, eps=1e-5):
    m = x.mean(dim=axis, keepdim=True)
    v = x.var(dim=axis, keepdim=True, unbiased=False)
    return (x - m) / torch.sqrt(v + eps)
def _scatter_add(tgt, idx, src, axis=0):
    return tgt.clone().index_add_(axis, idx, src)
def _segment_sum(src, seg, n):
    out = torch.zeros((n,) + src.shape[1:], dtype=src.dtype)
    return out.index_add_(0, seg, src)
def _pad(x, pw):
    flat = tuple(v for pair in reversed(pw) for v in pair)
    return torch.nn.functional.pad(x, flat)

kernel_name = 'concat_heads__mha_block_cached__gqa_block_cached__near_ridge'

def setup_inputs():

    _w = (lambda a: (lambda t: t.to(torch.bfloat16) if t.is_floating_point() else t)(torch.as_tensor(a)))
    def _A_setup():
        B, Sq, D = 16, 48, 640
        H, Sk, Dh = 32, 48, 128
        return {
            "x": _w((lambda *s: torch.randn(*s, dtype=torch.bfloat16) * 0.8)(B, Sq, D)),
            "k_cache": _w((lambda *s: torch.randn(*s, dtype=torch.bfloat16) * 0.8)(B, H, Sk, Dh)),
            "v_cache": _w((lambda *s: torch.randn(*s, dtype=torch.bfloat16) * 0.05)(B, H, Sk, Dh)),
            "wq": _w((lambda *s: torch.randn(*s, dtype=torch.bfloat16) * 0.8)(D, H * Dh) * (D ** -0.5)),
            "wo": _w((lambda *s: torch.randn(*s, dtype=torch.bfloat16) * 0.05)(H * Dh, D) * ((H * Dh) ** -0.5)),
        }
    _A_in = _A_setup()

    def _B_setup():
        B, Sq, D = 16, 48, 640
        Hq, Hkv, Sk, Dh = 32, 32, 48, 128
        assert Hq % Hkv == 0, (Hq, Hkv)
        return {
            "x": _w((lambda *s: torch.randn(*s, dtype=torch.bfloat16) * 0.8)(B, Sq, D)),
            "k_cache": _w((lambda *s: torch.randn(*s, dtype=torch.bfloat16) * 0.8)(B, Hkv, Sk, Dh)),
            "v_cache": _w((lambda *s: torch.randn(*s, dtype=torch.bfloat16) * 0.05)(B, Hkv, Sk, Dh)),
            "wq": _w((lambda *s: torch.randn(*s, dtype=torch.bfloat16) * 0.8)(D, Hq * Dh) * (D ** -0.5)),
            "wo": _w((lambda *s: torch.randn(*s, dtype=torch.bfloat16) * 0.05)(Hq * Dh, D) * ((Hq * Dh) ** -0.5)),
        }
    _B_in = _B_setup()

    return {
        "A_x": _A_in["x"],
        "A_k_cache": _A_in["k_cache"],
        "A_v_cache": _A_in["v_cache"],
        "A_wq": _A_in["wq"],
        "A_wo": _A_in["wo"],
        "B_x": _B_in["x"],
        "B_k_cache": _B_in["k_cache"],
        "B_v_cache": _B_in["v_cache"],
        "B_wq": _B_in["wq"],
        "B_wo": _B_in["wo"],
    }

def reference(A_x, A_k_cache, A_v_cache, A_wq, A_wo, B_x, B_k_cache, B_v_cache, B_wq, B_wo):
    def _A_ref(x, k_cache, v_cache, wq, wo):
        B, Sq, D = x.shape
        H, Dh = 32, 128
        q = (lambda x, *d: x.permute(*d))((x @ wq).reshape(B, Sq, H, Dh), 0, 2, 1, 3)
        scores = torch.einsum("bhqd,bhkd->bhqk", q, k_cache) * (Dh ** -0.5)
        p = _softmax(scores, axis=-1)
        o = torch.einsum("bhqk,bhkd->bhqd", p, v_cache)
        return (lambda x, *d: x.permute(*d))(o, 0, 2, 1, 3).reshape(B, Sq, H * Dh) @ wo

    def _B_ref(x, k_cache, v_cache, wq, wo):
        B, Sq, D = x.shape
        Hq, Dh = 32, 128
        Hkv = k_cache.shape[1]
        rep = Hq // Hkv
        q = (lambda x, *d: x.permute(*d))((x @ wq).reshape(B, Sq, Hq, Dh), 0, 2, 1, 3)
        k = (lambda x, n, axis: torch.repeat_interleave(x, n, dim=axis))(k_cache, rep, axis=1)
        v = (lambda x, n, axis: torch.repeat_interleave(x, n, dim=axis))(v_cache, rep, axis=1)
        scores = torch.einsum("bhqd,bhkd->bhqk", q, k) * (Dh ** -0.5)
        p = _softmax(scores, axis=-1)
        o = torch.einsum("bhqk,bhkd->bhqd", p, v)
        return (lambda x, *d: x.permute(*d))(o, 0, 2, 1, 3).reshape(B, Sq, Hq * Dh) @ wo

    a = _A_ref(A_x, A_k_cache, A_v_cache, A_wq, A_wo)
    b = _B_ref(B_x, B_k_cache, B_v_cache, B_wq, B_wo)
    return torch.concatenate([a, b], axis=-1)
```

```cpp
#include <hip/hip_runtime.h>
#include <hip/hip_bf16.h>

typedef __bf16 bf16x8 __attribute__((ext_vector_type(8)));
typedef __bf16 bf16x4 __attribute__((ext_vector_type(4)));
typedef short s16x4 __attribute__((ext_vector_type(4)));
typedef float f32x4 __attribute__((ext_vector_type(4)));
typedef unsigned u32x4 __attribute__((ext_vector_type(4)));
typedef unsigned u32x2 __attribute__((ext_vector_type(2)));
#define LAS __attribute__((address_space(3)))
#define GAS __attribute__((address_space(1)))
#define DI __device__ __forceinline__

constexpr int NB = 16, SQ = 48, DM = 640, NH = 32, DH = 128, SK = 48;
constexpr int MROWS = NB * SQ;
constexpr int HD = NH * DH;

struct Ptrs {
  const unsigned short* x[2];
  const unsigned short* k[2];
  const unsigned short* v[2];
  const unsigned short* wq[2];
  const unsigned short* wo[2];
  unsigned short* out;
  unsigned char* ws;
};

constexpr size_t OF_BYTES = (size_t)2 * 48 * 128 * 1024;
constexpr size_t PART_OFF = OF_BYTES;
constexpr size_t PART_BYTES = (size_t)2 * 4 * 48 * 40 * 1024;

DI void glds16(const unsigned char* sbase, unsigned voff, unsigned lds) {
  asm volatile("s_mov_b32 m0, %2\n\ts_nop 0\n\tglobal_load_lds_dwordx4 %0, %1" : : "v"(voff), "s"(sbase), "s"(lds) : "memory");
}
DI bf16x4 tr_read(LAS unsigned char* p) {
  return __builtin_amdgcn_ds_read_tr16_b64_v4bf16((LAS bf16x4*)p);
}
DI bf16x8 cat8(bf16x4 a, bf16x4 b) { return __builtin_shufflevector(a, b, 0, 1, 2, 3, 4, 5, 6, 7); }
DI bf16x8 pack8(f32x4 a, f32x4 b) {
  return cat8(__builtin_convertvector(a, bf16x4), __builtin_convertvector(b, bf16x4));
}
DI f32x4 mfma32(bf16x8 a, bf16x8 b, f32x4 c) { return __builtin_amdgcn_mfma_f32_16x16x32_bf16(a, b, c, 0, 0, 0); }
DI f32x4 mfma16(bf16x4 a, bf16x4 b, f32x4 c) {
  return __builtin_amdgcn_mfma_f32_16x16x16bf16_1k(__builtin_bit_cast(s16x4, a), __builtin_bit_cast(s16x4, b), c, 0, 0, 0);
}
DI float xor16_max(float v) {
  auto r = __builtin_amdgcn_permlane16_swap(__float_as_uint(v), __float_as_uint(v), false, false);
  return fmaxf(__uint_as_float(r[0]), __uint_as_float(r[1]));
}
DI float xor32_max(float v) {
  auto r = __builtin_amdgcn_permlane32_swap(__float_as_uint(v), __float_as_uint(v), false, false);
  return fmaxf(__uint_as_float(r[0]), __uint_as_float(r[1]));
}
DI float xor16_add(float v) {
  auto r = __builtin_amdgcn_permlane16_swap(__float_as_uint(v), __float_as_uint(v), false, false);
  return __uint_as_float(r[0]) + __uint_as_float(r[1]);
}
DI float xor32_add(float v) {
  auto r = __builtin_amdgcn_permlane32_swap(__float_as_uint(v), __float_as_uint(v), false, false);
  return __uint_as_float(r[0]) + __uint_as_float(r[1]);
}
#define WAIT_V(n) asm volatile("s_waitcnt vmcnt(" #n ")" ::: "memory")
#define BAR() __builtin_amdgcn_s_barrier()

constexpr int K1_STAGE = 40960;
constexpr int K1_LDS = 4 * K1_STAGE;

__global__ __launch_bounds__(256, 1) void k1_qattn(Ptrs P) {
  extern __shared__ __attribute__((aligned(16))) unsigned char smem[];
  LAS unsigned char* L = (LAS unsigned char*)smem;
  const int tid = threadIdx.x, lane = tid & 63;
  const int w = __builtin_amdgcn_readfirstlane(tid >> 6);
  const int bid = blockIdx.x, xcd = bid & 7, idx = bid >> 3;
  const int br = xcd >> 2, head = (xcd & 3) * 8 + (idx >> 2), mg = idx & 3;
  const int b = mg * 4 + w;
  const int g = lane >> 4, r16 = lane & 15, q4 = r16 >> 2, p4 = lane & 3;

  const unsigned char* xg = (const unsigned char*)P.x[br];
  const unsigned char* wqg = (const unsigned char*)P.wq[br];
  const unsigned char* kg = (const unsigned char*)P.k[br] + (size_t)(b * NH + head) * (SK * DH * 2);
  const unsigned char* vg = (const unsigned char*)P.v[br] + (size_t)(b * NH + head) * (SK * DH * 2);

  const unsigned lbase = (unsigned)(uintptr_t)L;
  const int xslot = lane & 7, xrow = lane >> 3;
  unsigned xv[2];
#pragma unroll
  for (int e = 0; e < 2; ++e) xv[e] = xrow * (DM * 2) + 16 * (xslot ^ (xrow >> 1) ^ (4 * e));
  const unsigned char* xs = xg + (size_t)(b * SQ) * (DM * 2);
  const int wsub = lane >> 5, wrow = (lane >> 2) & 7, wslot = lane & 3;
  unsigned wv[2];
#pragma unroll
  for (int e = 0; e < 2; ++e) wv[e] = wrow * (HD * 2) + 16 * (4 * wsub + (wslot ^ (2 * e + (wrow >> 2))));
  const unsigned char* wsb = wqg + (size_t)(16 * w) * (HD * 2) + head * 256;

  auto issue_stage = [&](int s) {
    const unsigned B = lbase + (s & 3) * K1_STAGE;
#pragma unroll
    for (int i = 0; i < 6; ++i) glds16(xs + (8 * i) * (DM * 2) + s * 128, xv[i & 1], B + w * 6144 + i * 1024);
#pragma unroll
    for (int i = 0; i < 4; ++i)
      glds16(wsb + (size_t)(s * 64 + 8 * (i >> 1)) * (HD * 2) + 128 * (i & 1), wv[i >> 1], B + 24576 + (4 * w + i) * 1024);
  };

  u32x4 kv[24];
  auto issue_kv = [&](int i) {
    const unsigned char* src = (i < 12 ? kg + i * 1024 : vg + (i - 12) * 1024) + lane * 16;
    kv[i] = *(const GAS u32x4*)src;
  };

  f32x4 acc[3][8];
#pragma unroll
  for (int m = 0; m < 3; ++m)
#pragma unroll
    for (int c = 0; c < 8; ++c) acc[m][c] = f32x4{0.f, 0.f, 0.f, 0.f};

  issue_stage(0);
  asm volatile("" ::: "memory");
  issue_stage(1);
  asm volatile("" ::: "memory");
  issue_stage(2);
  issue_kv(0); issue_kv(1); issue_kv(2);
  asm volatile("" ::: "memory");

  const int xoff0 = w * 6144 + r16 * 128 + 16 * ((0 + g) ^ ((r16 >> 1) & 7));
  const int xoff1 = w * 6144 + r16 * 128 + 16 * ((4 + g) ^ ((r16 >> 1) & 7));
  int woff[2][2];
#pragma unroll
  for (int c0 = 0; c0 < 2; ++c0)
#pragma unroll
    for (int u = 0; u < 2; ++u)
      woff[c0][u] = 24576 + 2048 * g + 64 * (4 * u + q4) + 16 * (2 * (c0 ^ (g & 1)) + ((p4 >> 1) ^ u)) + 8 * (p4 & 1);

#pragma unroll
  for (int s = 0; s < 10; ++s) {
    if (s == 0) WAIT_V(23);
    else if (s <= 7) WAIT_V(26);
    else if (s == 8) WAIT_V(13);
    else WAIT_V(0);
    asm volatile("s_waitcnt lgkmcnt(0)" ::: "memory");
    BAR();
    if (s + 3 < 10) {
      issue_stage(s + 3);
      issue_kv(3 + 3 * s); issue_kv(4 + 3 * s); issue_kv(5 + 3 * s);
    }
    asm volatile("" ::: "memory");
    LAS unsigned char* B = L + (s & 3) * K1_STAGE;
#pragma unroll
    for (int ks = 0; ks < 2; ++ks) {
      bf16x8 xf[3];
#pragma unroll
      for (int m = 0; m < 3; ++m) xf[m] = *(LAS bf16x8*)(B + (ks ? xoff1 : xoff0) + m * 2048);
#pragma unroll
      for (int c = 0; c < 8; ++c) {
        bf16x4 lo = tr_read(B + woff[c & 1][0] + 8192 * ks + 512 * (c >> 1));
        bf16x4 hi = tr_read(B + woff[c & 1][1] + 8192 * ks + 512 * (c >> 1));
        bf16x8 wf = cat8(lo, hi);
#pragma unroll
        for (int m = 0; m < 3; ++m) acc[m][c] = mfma32(wf, xf[m], acc[m][c]);
      }
    }
  }
  asm volatile("s_waitcnt lgkmcnt(0)" ::: "memory");
  BAR();

  LAS unsigned char* KB = L + w * 24576;
  LAS unsigned char* VB = KB + 12288;
#pragma unroll
  for (int i = 0; i < 12; ++i) {
    const int row = 4 * i + g;
    *(LAS u32x4*)(KB + row * 256 + 16 * (r16 ^ (row & 15))) = kv[i];
  }
#pragma unroll
  for (int i = 0; i < 12; ++i) {
    const int row = 4 * i + g;
    *(LAS u32x4*)(VB + row * 256 + 16 * (r16 ^ (((row & 3) << 2) | (((row >> 2) & 1) << 1)))) = kv[12 + i];
  }

  bf16x8 qf[3][4];
#pragma unroll
  for (int m = 0; m < 3; ++m)
#pragma unroll
    for (int s = 0; s < 4; ++s) qf[m][s] = pack8(acc[m][2 * s], acc[m][2 * s + 1]);

  f32x4 sc[3][3];
#pragma unroll
  for (int st = 0; st < 3; ++st)
#pragma unroll
    for (int m = 0; m < 3; ++m) sc[st][m] = f32x4{0.f, 0.f, 0.f, 0.f};
#pragma unroll
  for (int s = 0; s < 4; ++s) {
#pragma unroll
    for (int st = 0; st < 3; ++st) {
      const int row = 16 * st + r16;
      bf16x4 lo = *(LAS bf16x4*)(KB + row * 256 + 16 * ((4 * s + (g >> 1)) ^ r16) + 8 * (g & 1));
      bf16x4 hi = *(LAS bf16x4*)(KB + row * 256 + 16 * ((4 * s + 2 + (g >> 1)) ^ r16) + 8 * (g & 1));
      bf16x8 kf = cat8(lo, hi);
#pragma unroll
      for (int m = 0; m < 3; ++m) sc[st][m] = mfma32(kf, qf[m][s], sc[st][m]);
    }
  }
  const float cexp = 0.08838834764831845f * 1.4426950408889634f;
  bf16x8 pf[3];
  bf16x4 pl[3];
#pragma unroll
  for (int m = 0; m < 3; ++m) {
    float mx = sc[0][m][0];
#pragma unroll
    for (int st = 0; st < 3; ++st)
#pragma unroll
      for (int r = 0; r < 4; ++r) mx = fmaxf(mx, sc[st][m][r]);
    mx = xor16_max(mx);
    mx = xor32_max(mx);
    const float mb = mx * cexp;
    float sum = 0.f;
#pragma unroll
    for (int st = 0; st < 3; ++st)
#pragma unroll
      for (int r = 0; r < 4; ++r) {
        const float e = __builtin_amdgcn_exp2f(sc[st][m][r] * cexp - mb);
        sc[st][m][r] = e;
        sum += e;
      }
    sum = xor16_add(sum);
    sum = xor32_add(sum);
    const float inv = 1.0f / sum;
#pragma unroll
    for (int st = 0; st < 3; ++st) sc[st][m] *= inv;
    pf[m] = pack8(sc[0][m], sc[1][m]);
    pl[m] = __builtin_convertvector(sc[2][m], bf16x4);
  }
  {
    const int vsw = (q4 << 2) | ((g & 1) << 1);
    unsigned char* og = P.ws + ((size_t)(br * 48 + b * 3) * 128 + head * 4) * 1024 + lane * 16;
#pragma unroll
    for (int s = 0; s < 4; ++s) {
      f32x4 o[2][3];
#pragma unroll
      for (int cc = 0; cc < 2; ++cc) {
        const int c = 2 * s + cc;
        const int a = (4 * g + q4) * 256 + 16 * ((2 * c + (p4 >> 1)) ^ vsw) + 8 * (p4 & 1);
        bf16x4 v0 = tr_read(VB + a);
        bf16x4 v1 = tr_read(VB + a + 4096);
        bf16x4 v2 = tr_read(VB + a + 8192);
        bf16x8 v01 = cat8(v0, v1);
#pragma unroll
        for (int m = 0; m < 3; ++m) {
          f32x4 t = mfma32(v01, pf[m], f32x4{0.f, 0.f, 0.f, 0.f});
          o[cc][m] = mfma16(v2, pl[m], t);
        }
      }
#pragma unroll
      for (int m = 0; m < 3; ++m) {
        bf16x8 of = pack8(o[0][m], o[1][m]);
        *(GAS bf16x8*)(og + (size_t)m * 128 * 1024 + s * 1024) = of;
      }
    }
  }
}

constexpr int K2_LDS = 1024 * 160;

DI void gload16(u32x4& d, const unsigned char* sbase, unsigned voff, int imm) {
  asm volatile("global_load_dwordx4 %0, %1, %2 offset:%3" : "=v"(d) : "v"(voff), "s"(sbase), "i"(imm) : "memory");
}

__global__ __launch_bounds__(256, 1) void k2_oproj(Ptrs P) {
  extern __shared__ __attribute__((aligned(16))) unsigned char smem[];
  LAS unsigned char* L = (LAS unsigned char*)smem;
  const int tid = threadIdx.x, lane = tid & 63;
  const int w = __builtin_amdgcn_readfirstlane(tid >> 6);
  const int bid = blockIdx.x, xcd = bid & 7, idx = bid >> 3;
  const int br = xcd >> 2, kq = xcd & 3, nt = idx >> 2, mg = idx & 3;
  const int b = mg * 4 + w;
  const int g = lane >> 4, r16 = lane & 15, q4 = r16 >> 2, p4 = lane & 3;

  const unsigned char* wog = (const unsigned char*)P.wo[br] + (size_t)(kq * 1024) * (DM * 2) + nt * 160;
  const unsigned lbase = (unsigned)(uintptr_t)L;
  unsigned wsrc[5];
#pragma unroll
  for (int i = 0; i < 5; ++i) {
    const int p = (5 * w + i) * 64 + lane;
    wsrc[i] = (p / 10) * (DM * 2) + (p % 10) * 16;
  }
  const unsigned char* obase[3];
#pragma unroll
  for (int m = 0; m < 3; ++m) obase[m] = P.ws + ((size_t)(br * 48 + b * 3 + m) * 128 + kq * 32) * 1024;
  const unsigned lane16 = lane * 16;

  u32x4 of[3][4][3];
  auto issue = [&](int j, u32x4 (&dst)[4][3]) {
#pragma unroll
    for (int i = 0; i < 5; ++i)
      glds16(wog + (size_t)j * 128 * (DM * 2), wsrc[i], lbase + j * 20480 + (5 * w + i) * 1024);
#pragma unroll
    for (int ks = 0; ks < 4; ++ks)
#pragma unroll
      for (int m = 0; m < 3; ++m) gload16(dst[ks][m], obase[m], lane16 + j * 4096, ks * 1024);
  };

  f32x4 acc[3][5];
#pragma unroll
  for (int m = 0; m < 3; ++m)
#pragma unroll
    for (int c = 0; c < 5; ++c) acc[m][c] = f32x4{0.f, 0.f, 0.f, 0.f};

  issue(0, of[0]);
  issue(1, of[1]);
  issue(2, of[2]);
  const int woff = (4 * g + q4) * 160 + 8 * p4;

#define PIN(a) "+v"(a[0][0]), "+v"(a[0][1]), "+v"(a[0][2]), "+v"(a[1][0]), "+v"(a[1][1]), "+v"(a[1][2]), \
               "+v"(a[2][0]), "+v"(a[2][1]), "+v"(a[2][2]), "+v"(a[3][0]), "+v"(a[3][1]), "+v"(a[3][2])
#pragma unroll
  for (int j = 0; j < 8; ++j) {
    u32x4(&cur)[4][3] = of[j % 3];
    if (j <= 5) asm volatile("s_waitcnt vmcnt(34)" : PIN(cur) : : "memory");
    else if (j == 6) asm volatile("s_waitcnt vmcnt(17)" : PIN(cur) : : "memory");
    else asm volatile("s_waitcnt vmcnt(0)" : PIN(cur) : : "memory");
    BAR();
    LAS unsigned char* B = L + j * 20480 + woff;
#pragma unroll
    for (int ks = 0; ks < 4; ++ks) {
#pragma unroll
      for (int c = 0; c < 5; ++c) {
        bf16x4 lo = tr_read(B + ks * 5120 + 32 * c);
        bf16x4 hi = tr_read(B + ks * 5120 + 2560 + 32 * c);
        bf16x8 wf = cat8(lo, hi);
#pragma unroll
        for (int m = 0; m < 3; ++m) acc[m][c] = mfma32(wf, __builtin_bit_cast(bf16x8, cur[ks][m]), acc[m][c]);
      }
    }
    if (j + 3 < 8) {
      __builtin_amdgcn_sched_barrier(0);
      issue(j + 3, cur);
    }
  }
  unsigned char* pg = P.ws + PART_OFF + ((size_t)((br * 4 + kq) * 48 + b * 3) * 40 + nt * 5) * 1024 + lane * 16;
#pragma unroll
  for (int m = 0; m < 3; ++m)
#pragma unroll
    for (int c = 0; c < 5; ++c) *(GAS f32x4*)(pg + (m * 40 + c) * 1024) = acc[m][c];
}

__global__ __launch_bounds__(256) void k3_reduce(Ptrs P) {
  const int lane = threadIdx.x & 63;
  const int f = blockIdx.x * 4 + (threadIdx.x >> 6);
  const int br = f / 1920, rem = f % 1920, mt = rem / 40, n16 = rem % 40;
  const unsigned char* pg = P.ws + PART_OFF + ((size_t)(br * 4 * 48 + mt) * 40 + n16) * 1024 + lane * 16;
  f32x4 s0 = *(const GAS f32x4*)(pg);
  f32x4 s1 = *(const GAS f32x4*)(pg + (size_t)1 * 1920 * 1024);
  f32x4 s2 = *(const GAS f32x4*)(pg + (size_t)2 * 1920 * 1024);
  f32x4 s3 = *(const GAS f32x4*)(pg + (size_t)3 * 1920 * 1024);
  f32x4 s = (s0 + s1) + (s2 + s3);
  bf16x4 o = __builtin_convertvector(s, bf16x4);
  const int m = mt * 16 + (lane & 15), n = n16 * 16 + 4 * (lane >> 4);
  *(GAS bf16x4*)(P.out + (size_t)m * 1280 + br * 640 + n) = o;
}

extern "C" void kernel_launch(void* const* d_in, const int* in_sizes, int n_in, void* d_out, int out_size,
                              void* d_ws, size_t ws_size, hipStream_t stream) {
  Ptrs P;
  for (int i = 0; i < 2; ++i) {
    P.x[i] = (const unsigned short*)d_in[5 * i + 0];
    P.k[i] = (const unsigned short*)d_in[5 * i + 1];
    P.v[i] = (const unsigned short*)d_in[5 * i + 2];
    P.wq[i] = (const unsigned short*)d_in[5 * i + 3];
    P.wo[i] = (const unsigned short*)d_in[5 * i + 4];
  }
  P.out = (unsigned short*)d_out;
  P.ws = (unsigned char*)d_ws;
  (void)hipFuncSetAttribute((const void*)k1_qattn, hipFuncAttributeMaxDynamicSharedMemorySize, K1_LDS);
  (void)hipFuncSetAttribute((const void*)k2_oproj, hipFuncAttributeMaxDynamicSharedMemorySize, K2_LDS);
  k1_qattn<<<256, 256, K1_LDS, stream>>>(P);
  k2_oproj<<<256, 256, K2_LDS, stream>>>(P);
  k3_reduce<<<960, 256, 0, stream>>>(P);
}
```

```cpp
#include <hip/hip_runtime.h>
#include <hip/hip_bf16.h>
#include <type_traits>

typedef __bf16 bf16x8 __attribute__((ext_vector_type(8)));
typedef __bf16 bf16x4 __attribute__((ext_vector_type(4)));
typedef float f32x4 __attribute__((ext_vector_type(4)));
typedef unsigned u32x4 __attribute__((ext_vector_type(4)));
typedef unsigned u32x2 __attribute__((ext_vector_type(2)));
typedef unsigned long long u64;
#define LAS __attribute__((address_space(3)))
#define GAS __attribute__((address_space(1)))
#define DI __device__ __forceinline__
#define RLX_AGENT __ATOMIC_RELAXED, __HIP_MEMORY_SCOPE_AGENT

constexpr int SQ = 48, DM = 640, NH = 32, DH = 128, SK = 48;
constexpr int HD = NH * DH;

struct Ptrs {
  const unsigned short* x[2];
  const unsigned short* k[2];
  const unsigned short* v[2];
  const unsigned short* wq[2];
  const unsigned short* wo[2];
  unsigned short* out;
  unsigned char* ws;
};

constexpr int Q_OFF = 0;
constexpr int A_OFF = 1024;
constexpr int E_OFF = 2048;
constexpr int F1_OFF = 4096;
constexpr int F2_OFF = F1_OFF + 128 * 128;
constexpr int CTRL_BYTES = 65536;
constexpr size_t OF_OFF = CTRL_BYTES;
constexpr size_t OF_BR = (size_t)48 * 128 * 1024;
constexpr size_t PART_OFF = OF_OFF + 2 * OF_BR;
constexpr unsigned SPIN_CAP = 1u << 18;
constexpr int LEADER = 255;

#define POL_X ""
#define POL_W ""
#define W2THROTTLE 0
#define OAUX 16
#define POL_O " sc1"
#define POL_KV " nt"

#define GLDS(pol, sbase, voff, lds) \
  asm volatile("s_mov_b32 m0, %2\n\ts_nop 0\n\tglobal_load_lds_dwordx4 %0, %1" pol : : "v"(voff), "s"(sbase), "s"(lds) : "memory")

DI bf16x4 tr_read(LAS unsigned char* p) { return __builtin_amdgcn_ds_read_tr16_b64_v4bf16((LAS bf16x4*)p); }
DI bf16x8 cat8(bf16x4 a, bf16x4 b) { return __builtin_shufflevector(a, b, 0, 1, 2, 3, 4, 5, 6, 7); }
DI bf16x8 pack8(f32x4 a, f32x4 b) { return cat8(__builtin_convertvector(a, bf16x4), __builtin_convertvector(b, bf16x4)); }
DI f32x4 mfma32(bf16x8 a, bf16x8 b, f32x4 c) { return __builtin_amdgcn_mfma_f32_16x16x32_bf16(a, b, c, 0, 0, 0); }
DI float xor16_max(float v) {
  auto r = __builtin_amdgcn_permlane16_swap(__float_as_uint(v), __float_as_uint(v), false, false);
  return fmaxf(__uint_as_float(r[0]), __uint_as_float(r[1]));
}
DI float xor32_max(float v) {
  auto r = __builtin_amdgcn_permlane32_swap(__float_as_uint(v), __float_as_uint(v), false, false);
  return fmaxf(__uint_as_float(r[0]), __uint_as_float(r[1]));
}
DI float xor16_add(float v) {
  auto r = __builtin_amdgcn_permlane16_swap(__float_as_uint(v), __float_as_uint(v), false, false);
  return __uint_as_float(r[0]) + __uint_as_float(r[1]);
}
DI float xor32_add(float v) {
  auto r = __builtin_amdgcn_permlane32_swap(__float_as_uint(v), __float_as_uint(v), false, false);
  return __uint_as_float(r[0]) + __uint_as_float(r[1]);
}
#define STAMP(i)
#define WAIT_V(n) asm volatile("s_waitcnt vmcnt(%0)" ::"i"(n) : "memory")
#define WAIT_L0() asm volatile("s_waitcnt lgkmcnt(0)" ::: "memory")
#define BAR() do { asm volatile("" ::: "memory"); __builtin_amdgcn_s_barrier(); asm volatile("" ::: "memory"); } while (0)
#define SCHED0() __builtin_amdgcn_sched_barrier(0)

constexpr int STAGE = 40960;
constexpr int LDS_BYTES = 4 * STAGE;
constexpr int XWORD = 118784;
DI int kv_slot(int t) { return t < 10 ? 3 * STAGE + 4096 * t : t < 20 ? STAGE + 4096 * (t - 10) : 2 * STAGE + 4096 * (t - 20); }
DI int w2_slot(int j7) { return (int)((0x7982106u >> (4 * j7)) & 15) * 16384; }
constexpr int O_RING = 3 * 16384;

__global__ __launch_bounds__(512, 2) void fused(Ptrs P) {
  extern __shared__ __attribute__((aligned(16))) unsigned char smem[];
  LAS unsigned char* L = (LAS unsigned char*)smem;
  const int tid = threadIdx.x, lane = tid & 63;
  const int wv = __builtin_amdgcn_readfirstlane(tid >> 6);
  const int w = wv & 3;
  const int bid = blockIdx.x, xcd = bid & 7, idx = bid >> 3;
  const int br = xcd >> 2, kq = xcd & 3, hl = idx >> 2, head = kq * 8 + hl, mg = idx & 3;
  const bool p2 = idx < 30;
  const int mt = idx / 5, nt = idx % 5;
  const int g = lane >> 4, r16 = lane & 15, q4 = r16 >> 2, p4 = lane & 3;
#define ROT2 (mt * 5 + nt * 3)
  const int rot2 = ROT2;

  if (wv >= 4) {
    const unsigned lbase = (unsigned)(uintptr_t)L;
    const int xslot = lane & 7, xrow = lane >> 3;
    const unsigned xv0 = xrow * (DM * 2) + 16 * (xslot ^ (xrow >> 1));
    const unsigned xv1 = xv0 ^ 64;
    const unsigned char* xs = (const unsigned char*)P.x[br] + (size_t)((mg * 4 + w) * SQ) * (DM * 2);
    const int wsub = lane >> 5, wrow = (lane >> 2) & 7, wslot = lane & 3;
    const unsigned wv0 = wrow * (HD * 2) + 16 * (4 * wsub + (wslot ^ (wrow >> 2)));
    const unsigned wv1 = wv0 ^ 32;
    const unsigned char* wsb = (const unsigned char*)P.wq[br] + (size_t)(16 * w) * (HD * 2) + head * 256;
#define ROT1 (hl + ((mg & 1) ? 5 : 0) + ((mg & 2) ? 3 : 0))
    int c = ROT1;
    c -= (c >= 10) ? 10 : 0;
    unsigned buf = lbase;
    auto issue_stage = [&]() {
      const unsigned char* xc = xs + c * 128;
      const unsigned char* wc = wsb + (size_t)c * 64 * (HD * 2);
      GLDS(POL_W, wc, wv0, buf + 24576 + (4 * w + 0) * 1024);
      GLDS(POL_W, wc + 128, wv0, buf + 24576 + (4 * w + 1) * 1024);
      GLDS(POL_W, wc + 8 * (HD * 2), wv1, buf + 24576 + (4 * w + 2) * 1024);
      GLDS(POL_W, wc + 8 * (HD * 2) + 128, wv1, buf + 24576 + (4 * w + 3) * 1024);
#pragma unroll
      for (int i = 0; i < 6; ++i) GLDS(POL_X, xc + (8 * i) * (DM * 2), (i & 1) ? xv1 : xv0, buf + w * 6144 + i * 1024);
      c = (c == 9) ? 0 : c + 1;
      buf = (buf == lbase + 2 * STAGE) ? lbase : buf + STAGE;
    };
    const unsigned kvo = (4 * w + g) * 256;
    const unsigned kvK = kvo + 16 * (r16 ^ (4 * w + g));
    const unsigned kvV = kvo + 16 * (r16 ^ ((g << 2) | ((w & 1) << 1)));
    const unsigned char* kg = (const unsigned char*)P.k[br] + (size_t)(mg * 4 * NH + head) * (SK * DH * 2);
    const unsigned char* vg = (const unsigned char*)P.v[br] + (size_t)(mg * 4 * NH + head) * (SK * DH * 2);
    auto issue_kv = [&](int t) {
      const int tt = t < 12 ? t : t - 12;
      const unsigned char* src = (t < 12 ? kg : vg) + (size_t)(tt / 3) * (NH * SK * DH * 2) + (tt % 3) * 4096;
      GLDS(POL_KV, src, t < 12 ? kvK : kvV, lbase + kv_slot(t) + w * 1024);
    };
    const unsigned char* wog = (const unsigned char*)P.wo[br] + (size_t)(kq * 1024 + 16 * w) * (DM * 2) + nt * 256;
    const unsigned w2a = wrow * (DM * 2) + 16 * (4 * wsub + (wslot ^ ((wrow >> 2) << 1)));
    int wj = 0, wj7 = 0;
    auto issue_w2 = [&]() {
      if (!p2) return;
      const unsigned char* wc = wog + (size_t)((wj + rot2) & 15) * 64 * (DM * 2);
      const unsigned B = lbase + w2_slot(wj7) + 4096 * w;
      GLDS(POL_W, wc, w2a, B);
      GLDS(POL_W, wc + 128, w2a, B + 1024);
      GLDS(POL_W, wc + 8 * (DM * 2), w2a, B + 2048);
      GLDS(POL_W, wc + 8 * (DM * 2) + 128, w2a, B + 3072);
      ++wj; wj7 = (wj7 == 6) ? 0 : wj7 + 1;
    };
    const int Ma = mt * 8 + ((2 * w) & 4) + ((2 * w + kq) & 3), Mb = mt * 8 + ((2 * w) & 4) + ((2 * w + 1 + kq) & 3);
    const unsigned char* oga = P.ws + OF_OFF + br * OF_BR + (size_t)(Ma * 128 + kq * 32) * 1024;
    const unsigned char* ogb = P.ws + OF_OFF + br * OF_BR + (size_t)(Mb * 128 + kq * 32) * 1024;
    int oj = 0, oj3 = 0;
    auto issue_o = [&]() {
      const int co = ((oj + rot2) & 15) * 2048;
      const unsigned B = lbase + O_RING + oj3 * 16384 + 4096 * w;
      GLDS(POL_O, oga + co, lane * 16, B);
      GLDS(POL_O, oga + co + 1024, lane * 16, B + 1024);
      GLDS(POL_O, ogb + co, lane * 16, B + 2048);
      GLDS(POL_O, ogb + co + 1024, lane * 16, B + 3072);
      ++oj; oj3 = (oj3 == 2) ? 0 : oj3 + 1;
    };
#define KB (1 + 2 * ((idx + xcd) & 1))
    const int kb = KB;

    const unsigned char* ep = P.ws + E_OFF + bid * 8;
    const unsigned vzero = 0;
    u32x2 e;
    asm volatile("global_load_dwordx2 %0, %1, %2 sc1" : "=v"(e) : "v"(vzero), "s"(ep) : "memory");
    issue_stage();
    asm volatile("s_waitcnt vmcnt(10)" : "+v"(e) : : "memory");
    const unsigned nonce = e[0] + 1u;
    if (tid == 256) asm volatile("global_store_dword %0, %1, %2 sc1" : : "v"(vzero), "v"(nonce), "s"(P.ws + Q_OFF + bid * 4) : "memory");
    issue_stage();
    const bool leader = (bid == LEADER) && (wv == 5);
    unsigned X = 1;
    bool all_acked = false;
    const unsigned lane16 = lane * 16;
    if (leader) {
      const __amdgpu_buffer_rsrc_t rc = __builtin_amdgcn_make_buffer_rsrc((void*)P.ws, 0, CTRL_BYTES, 0x00020000);
      u32x4 f[9];
      f[0] = __builtin_amdgcn_raw_buffer_load_b128(rc, A_OFF + lane * 16, 0, 16);
#pragma unroll
      for (int i = 0; i < 4; ++i) f[1 + i] = __builtin_amdgcn_raw_buffer_load_b128(rc, F1_OFF + ((lane + 64 * i) >> 1) * 128 + (lane & 1) * 16, 0, 16);
#pragma unroll
      for (int i = 0; i < 4; ++i) f[5 + i] = __builtin_amdgcn_raw_buffer_load_b128(rc, F2_OFF + min(lane + 64 * i, 239) * 128, 0, 16);
      while (true) {
        bool hit = false;
#pragma unroll
        for (int i = 0; i < 9; ++i) hit |= (f[i][0] == X) | (f[i][1] == X) | (f[i][2] == X) | (f[i][3] == X);
        if (!__any(hit)) break;
        ++X;
      }
    }
    u32x4 lq, la;
#define SERVE_LOAD() do { \
      asm volatile("global_load_dwordx4 %0, %1, %2 sc1" : "=v"(lq) : "v"(lane16), "s"(P.ws + Q_OFF) : "memory"); \
      asm volatile("global_load_dwordx4 %0, %1, %2 sc1" : "=v"(la) : "v"(lane16), "s"(P.ws + A_OFF) : "memory"); } while (0)
#define SERVE_WAIT(n) asm volatile("s_waitcnt vmcnt(%2)" : "+v"(lq), "+v"(la) : "i"(n) : "memory")
    auto serve_store = [&]() {
#pragma unroll
      for (int i = 0; i < 4; ++i) {
        const u32x2 ev = {lq[i], X};
        asm volatile("global_store_dwordx2 %0, %1, %2 offset:%3 sc1" : : "v"(lane16 * 2), "v"(ev), "s"(P.ws + E_OFF), "i"(i * 8) : "memory");
      }
      const bool ok = (la[0] == X) & (la[1] == X) & (la[2] == X) & ((la[3] == X) | (lane == 63));
      all_acked = __all(ok);
    };
    WAIT_V(10);
    BAR();
    issue_stage();
    WAIT_V(10);
    BAR();
    auto loop = [&](auto lead_c) {
      constexpr bool LEAD = decltype(lead_c)::value;
#pragma unroll 1
      for (int i = 0; i < 7; ++i) {
        if (i == kb) {
          if constexpr (LEAD) SERVE_LOAD();
#pragma unroll
          for (int t = 0; t < 10; ++t) issue_kv(t);
          issue_stage();
          if constexpr (LEAD) SERVE_WAIT(20); else WAIT_V(20);
        } else {
          if constexpr (LEAD) SERVE_LOAD();
          issue_stage();
          if constexpr (LEAD) SERVE_WAIT(10); else WAIT_V(10);
        }
        if constexpr (LEAD) serve_store();
        BAR();
      }
    };
    if (leader) loop(std::true_type{}); else loop(std::false_type{});
    asm volatile("global_load_dwordx2 %0, %1, %2 sc1" : "=v"(e) : "v"(vzero), "s"(ep) : "memory");
#pragma unroll
    for (int t = 10; t < 20; ++t) issue_kv(t);
    asm volatile("s_waitcnt vmcnt(10)" : "+v"(e) : : "memory");
    BAR();
#pragma unroll
    for (int t = 20; t < 24; ++t) issue_kv(t);
    WAIT_V(12);
    BAR();
    if (wv == 4 && bid != LEADER) {
      for (unsigned spins = 0; e[0] != nonce && spins < SPIN_CAP; ++spins) {
        __builtin_amdgcn_s_sleep(2);
        const u64 t = __hip_atomic_load((GAS u64*)(P.ws + E_OFF) + bid, RLX_AGENT);
        e[0] = (unsigned)t; e[1] = (unsigned)(t >> 32);
      }
      if (lane == 0) {
        asm volatile("global_store_dword %0, %1, %2 sc1" : : "v"(vzero), "v"(e[1]), "s"(P.ws + A_OFF + bid * 4) : "memory");
        *(volatile LAS unsigned*)(L + XWORD) = e[1];
      }
      WAIT_L0();
    }
    if (leader) {
      if (lane == 0) *(volatile LAS unsigned*)(L + XWORD) = X;
      WAIT_L0();
    }
    issue_w2(); issue_w2(); issue_w2();
    if (p2) WAIT_V(12); else WAIT_V(0);
    BAR();
    if (!p2) {
      if (leader)
        for (unsigned spins = 0; !all_acked && spins < SPIN_CAP; ++spins) { SERVE_LOAD(); SERVE_WAIT(0); serve_store(); __builtin_amdgcn_s_sleep(2); }
      return;
    }
    X = __builtin_amdgcn_readfirstlane(*(volatile LAS unsigned*)(L + XWORD));
    issue_w2();
    WAIT_L0();
    BAR();
#pragma unroll 1
    for (int j = 4; j < 7; ++j) { WAIT_V(W2THROTTLE); issue_w2(); }
    {
      const int bb = (lane & 8) ? Mb / 3 : Ma / 3;
      GAS unsigned* fp = (GAS unsigned*)P.ws + (F1_OFF + ((br * 16 + bb) * 4 + kq) * 128) / 4 + (lane & 7);
      for (unsigned spins = 0; spins < SPIN_CAP; ++spins) {
        const unsigned v = __hip_atomic_load(fp, RLX_AGENT);
        if (__all(v == X)) break;
        __builtin_amdgcn_s_sleep(1);
      }
    }
    issue_o(); issue_o();
    WAIT_V(4);
    BAR();
    issue_o();
    WAIT_V(4);
    BAR();
#pragma unroll 1
    for (int j = 0; j < 9; ++j) { issue_o(); issue_w2(); WAIT_V(8); BAR(); }
#pragma unroll 1
    for (int j = 9; j < 13; ++j) { issue_o(); WAIT_V(4); BAR(); }
    WAIT_V(0);
    BAR();
    return;
  }

  STAMP(0);
  GAS unsigned* ctl = (GAS unsigned*)P.ws;
  f32x4 acc[3][8];
#pragma unroll
  for (int m = 0; m < 3; ++m)
#pragma unroll
    for (int c = 0; c < 8; ++c) acc[m][c] = f32x4{0.f, 0.f, 0.f, 0.f};

  const int xoff0 = w * 6144 + r16 * 128 + 16 * (g ^ ((r16 >> 1) & 7));
  const int woff00 = 24576 + 2048 * g + 64 * q4 + 16 * (2 * (g & 1) + (p4 >> 1)) + 8 * (p4 & 1);
  bf16x8 xf[2][3], wf[2][8];
  auto read_frags = [&](int bufo, int ks, bf16x8 (&xd)[3], bf16x8 (&wd)[8]) {
    LAS unsigned char* xb = L + bufo + (xoff0 ^ (64 * ks));
    LAS unsigned char* w00 = L + bufo + woff00 + 8192 * ks;
    LAS unsigned char* w01 = L + bufo + (woff00 ^ 16) + 256 + 8192 * ks;
    LAS unsigned char* w10 = L + bufo + (woff00 ^ 32) + 8192 * ks;
    LAS unsigned char* w11 = L + bufo + (woff00 ^ 48) + 256 + 8192 * ks;
#pragma unroll
    for (int m = 0; m < 3; ++m) xd[m] = *(LAS bf16x8*)(xb + m * 2048);
#pragma unroll
    for (int c = 0; c < 8; ++c) {
      bf16x4 lo = tr_read(((c & 1) ? w10 : w00) + 512 * (c >> 1));
      bf16x4 hi = tr_read(((c & 1) ? w11 : w01) + 512 * (c >> 1));
      wd[c] = cat8(lo, hi);
    }
  };
  auto mma = [&](bf16x8 (&xd)[3], bf16x8 (&wd)[8]) {
#pragma unroll
    for (int c = 0; c < 8; ++c)
#pragma unroll
      for (int m = 0; m < 3; ++m) acc[m][c] = mfma32(wd[c], xd[m], acc[m][c]);
  };
  auto interleave = [&]() {
#pragma unroll
    for (int i = 0; i < 19; ++i) {
      __builtin_amdgcn_sched_group_barrier(0x008, 1, 0);
      __builtin_amdgcn_sched_group_barrier(0x100, 1, 0);
    }
    __builtin_amdgcn_sched_group_barrier(0x008, 5, 0);
  };

  BAR();
  STAMP(1);
  read_frags(0, 0, xf[0], wf[0]);
  {
    int cur = 0;
#pragma unroll 1
    for (int s = 0; s < 10; ++s) {
      SCHED0();
      read_frags(cur, 1, xf[1], wf[1]);
      mma(xf[0], wf[0]);
      interleave();
      SCHED0();
      WAIT_L0();
      BAR();
      cur = (cur == 2 * STAGE) ? 0 : cur + STAGE;
      SCHED0();
      read_frags(cur, 0, xf[0], wf[0]);
      mma(xf[1], wf[1]);
      interleave();
    }
  }
  SCHED0();
  STAMP(2);

  bf16x8 pf[3], pl[3];
  const bf16x4 zero4 = __builtin_bit_cast(bf16x4, u32x2{0u, 0u});
  {
    bf16x8 kf[3][4];
#pragma unroll
    for (int st = 0; st < 3; ++st) {
      LAS unsigned char* KB_ = L + kv_slot(w * 3 + st) + r16 * 256 + 8 * (g & 1);
#pragma unroll
      for (int s = 0; s < 4; ++s) {
        bf16x4 lo = *(LAS bf16x4*)(KB_ + 16 * ((4 * s + (g >> 1)) ^ r16));
        bf16x4 hi = *(LAS bf16x4*)(KB_ + 16 * ((4 * s + 2 + (g >> 1)) ^ r16));
        kf[st][s] = cat8(lo, hi);
      }
    }
    const float cexp = 0.08838834764831845f * 1.4426950408889634f;
#pragma unroll
    for (int m = 0; m < 3; ++m) {
      f32x4 sc[3];
#pragma unroll
      for (int st = 0; st < 3; ++st) sc[st] = f32x4{0.f, 0.f, 0.f, 0.f};
#pragma unroll
      for (int s = 0; s < 4; ++s) {
        const bf16x8 qf = pack8(acc[m][2 * s], acc[m][2 * s + 1]);
#pragma unroll
        for (int st = 0; st < 3; ++st) sc[st] = mfma32(kf[st][s], qf, sc[st]);
      }
      float mx = sc[0][0];
#pragma unroll
      for (int st = 0; st < 3; ++st)
#pragma unroll
        for (int r = 0; r < 4; ++r) mx = fmaxf(mx, sc[st][r]);
      mx = xor16_max(mx);
      mx = xor32_max(mx);
      const float mb = mx * cexp;
#pragma unroll
      for (int st = 0; st < 3; ++st)
#pragma unroll
        for (int r = 0; r < 4; ++r) sc[st][r] = __builtin_amdgcn_exp2f(sc[st][r] * cexp - mb);
      const f32x4 s4 = (sc[0] + sc[1]) + sc[2];
      float sum = (s4[0] + s4[1]) + (s4[2] + s4[3]);
      sum = xor16_add(sum);
      sum = xor32_add(sum);
      const float inv = __builtin_amdgcn_rcpf(sum);
#pragma unroll
      for (int st = 0; st < 3; ++st) sc[st] *= inv;
      pf[m] = pack8(sc[0], sc[1]);
      pl[m] = cat8(__builtin_convertvector(sc[2], bf16x4), zero4);
    }
  }
  STAMP(3);
  WAIT_L0();
  BAR();
  STAMP(4);
  const unsigned X = __builtin_amdgcn_readfirstlane(*(volatile LAS unsigned*)(L + XWORD));
  const __amdgpu_buffer_rsrc_t ro = __builtin_amdgcn_make_buffer_rsrc((void*)(P.ws + OF_OFF + br * OF_BR), 0, (int)OF_BR, 0x00020000);
  {
    const int vsw = (q4 << 2) | ((g & 1) << 1);
    LAS unsigned char* V0 = L + kv_slot(12 + w * 3 + 0);
    LAS unsigned char* V1 = L + kv_slot(12 + w * 3 + 1);
    LAS unsigned char* V2 = L + kv_slot(12 + w * 3 + 2);
    bf16x8 v01[8], v2z[8];
#pragma unroll
    for (int c = 0; c < 8; ++c) {
      const int a = (4 * g + q4) * 256 + 16 * ((2 * c + (p4 >> 1)) ^ vsw) + 8 * (p4 & 1);
      v01[c] = cat8(tr_read(V0 + a), tr_read(V1 + a));
      v2z[c] = cat8(tr_read(V2 + a), zero4);
    }
    if (p2) {
      WAIT_L0();
      BAR();
    }
    const int obase = (((mg * 4 + w) * 3) * 128 + head * 4) * 1024;
#pragma unroll
    for (int s = 0; s < 4; ++s) {
#pragma unroll
      for (int m = 0; m < 3; ++m) {
        f32x4 o0 = mfma32(v01[2 * s], pf[m], f32x4{0.f, 0.f, 0.f, 0.f});
        f32x4 o1 = mfma32(v01[2 * s + 1], pf[m], f32x4{0.f, 0.f, 0.f, 0.f});
        o0 = mfma32(v2z[2 * s], pl[m], o0);
        o1 = mfma32(v2z[2 * s + 1], pl[m], o1);
        __builtin_amdgcn_raw_buffer_store_b128(__builtin_bit_cast(u32x4, pack8(o0, o1)), ro, lane * 16 + s * 1024 + obase + m * 131072, 0, 16);
      }
    }
  }
  STAMP(5);
  WAIT_V(0);
  if (lane == 0) __hip_atomic_store(ctl + (F1_OFF + ((br * 16 + mg * 4 + w) * 4 + kq) * 128) / 4 + hl, X, RLX_AGENT);
  STAMP(6);
  if (!p2) return;

  const int wm = w >> 1, wn = w & 1;
  f32x4 ac2[4][4];
#pragma unroll
  for (int m = 0; m < 4; ++m)
#pragma unroll
    for (int c = 0; c < 4; ++c) ac2[m][c] = f32x4{0.f, 0.f, 0.f, 0.f};
  const int ooff = O_RING + wm * 8192 + lane * 16;
  const int w2o = 2048 * (g >> 1) + 1024 * wn + 64 * (4 * (g & 1) + q4) + 16 * (2 * (g & 1) + (p4 >> 1)) + 8 * (p4 & 1);
  bf16x8 of[2][4], wg[2][4];
  auto read2 = [&](int oslot, int wslot_, int ks, bf16x8 (&od)[4], bf16x8 (&wd)[4]) {
    LAS unsigned char* ob = L + oslot + ooff + 1024 * ks;
    LAS unsigned char* b0 = L + wslot_ + w2o + 8192 * ks;
    LAS unsigned char* b1 = L + wslot_ + (w2o ^ 32) + 8192 * ks;
#pragma unroll
    for (int m = 0; m < 4; ++m) od[m] = *(LAS bf16x8*)(ob + m * 2048);
#pragma unroll
    for (int c = 0; c < 4; ++c) {
      LAS unsigned char* bp = ((c & 1) ? b1 : b0) + 512 * (c >> 1);
      wd[c] = cat8(tr_read(bp), tr_read(bp + 4096));
    }
  };
  auto mma2 = [&](bf16x8 (&od)[4], bf16x8 (&wd)[4]) {
#pragma unroll
    for (int c = 0; c < 4; ++c)
#pragma unroll
      for (int m = 0; m < 4; ++m) ac2[m][c] = mfma32(wd[c], od[m], ac2[m][c]);
  };
  auto inter2 = [&]() {
#pragma unroll
    for (int i = 0; i < 12; ++i) {
      __builtin_amdgcn_sched_group_barrier(0x008, 1, 0);
      __builtin_amdgcn_sched_group_barrier(0x100, 1, 0);
    }
    __builtin_amdgcn_sched_group_barrier(0x008, 4, 0);
  };
  STAMP(7);
  BAR();
  STAMP(8);
  read2(0, w2_slot(0), 0, of[0], wg[0]);
  {
    int j3 = 0, j7 = 0;
#pragma unroll 1
    for (int j = 0; j < 16; ++j) {
      SCHED0();
      read2(j3 * 16384, w2_slot(j7), 1, of[1], wg[1]);
      mma2(of[0], wg[0]);
      inter2();
      SCHED0();
      WAIT_L0();
      BAR();
      j3 = (j3 == 2) ? 0 : j3 + 1;
      j7 = (j7 == 6) ? 0 : j7 + 1;
      SCHED0();
      read2(j3 * 16384, w2_slot(j7), 0, of[0], wg[0]);
      mma2(of[1], wg[1]);
      inter2();
    }
  }
  SCHED0();
  STAMP(9);

  const int tile = br * 30 + idx;
  const __amdgpu_buffer_rsrc_t rs = __builtin_amdgcn_make_buffer_rsrc((void*)(P.ws + PART_OFF + (size_t)tile * (4 * 4 * 16 * 1024)), 0, 4 * 4 * 16 * 1024, 0x00020000);
#pragma unroll
  for (int j = 1; j < 4; ++j)
#pragma unroll
    for (int c = 0; c < 4; ++c)
      __builtin_amdgcn_raw_buffer_store_b128(__builtin_bit_cast(u32x4, ac2[j][c]), rs, lane * 16 + (j * 4 + c) * 1024 + (kq * 4 + w) * 16384, 0, 16);
  WAIT_V(0);
  GAS unsigned* f2 = ctl + (F2_OFF + (tile * 4 + w) * 128) / 4;
  if (lane == 0) __hip_atomic_store(f2 + kq, X, RLX_AGENT);
  STAMP(10);
  for (unsigned spins = 0; spins < SPIN_CAP; ++spins) {
    const unsigned v = __hip_atomic_load(f2 + (lane & 3), RLX_AGENT);
    if (__all(v == X)) break;
    __builtin_amdgcn_s_sleep(1);
  }
  __builtin_amdgcn_fence(__ATOMIC_ACQUIRE, "wavefront");
  STAMP(11);
  u32x4 pr[3][4];
#pragma unroll
  for (int j = 1; j < 4; ++j) {
    const int src = (kq - j) & 3;
#pragma unroll
    for (int c = 0; c < 4; ++c)
      pr[j - 1][c] = __builtin_amdgcn_raw_buffer_load_b128(rs, lane * 16 + (j * 4 + c) * 1024, (src * 4 + w) * 16384, 16);
  }
  unsigned short* orow = P.out + (size_t)(mt * 128 + wm * 64 + kq * 16 + r16) * 1280 + br * 640 + nt * 128 + wn * 64 + 4 * g;
#pragma unroll
  for (int c = 0; c < 4; ++c) {
    f32x4 s = (ac2[0][c] + __builtin_bit_cast(f32x4, pr[0][c])) + (__builtin_bit_cast(f32x4, pr[1][c]) + __builtin_bit_cast(f32x4, pr[2][c]));
    *(GAS bf16x4*)(orow + 16 * c) = __builtin_convertvector(s, bf16x4);
  }
  STAMP(12);
}

extern "C" void kernel_launch(void* const* d_in, const int* in_sizes, int n_in, void* d_out, int out_size,
                              void* d_ws, size_t ws_size, hipStream_t stream) {
  Ptrs P;
  for (int i = 0; i < 2; ++i) {
    P.x[i] = (const unsigned short*)d_in[5 * i + 0];
    P.k[i] = (const unsigned short*)d_in[5 * i + 1];
    P.v[i] = (const unsigned short*)d_in[5 * i + 2];
    P.wq[i] = (const unsigned short*)d_in[5 * i + 3];
    P.wo[i] = (const unsigned short*)d_in[5 * i + 4];
  }
  P.out = (unsigned short*)d_out;
  P.ws = (unsigned char*)d_ws;
  (void)hipFuncSetAttribute((const void*)fused, hipFuncAttributeMaxDynamicSharedMemorySize, LDS_BYTES);
  fused<<<256, 512, LDS_BYTES, stream>>>(P);
}
```

```cpp
#include <hip/hip_runtime.h>
#include <hip/hip_bf16.h>

typedef __bf16 bf16x8 __attribute__((ext_vector_type(8)));
typedef __bf16 bf16x4 __attribute__((ext_vector_type(4)));
typedef float f32x4 __attribute__((ext_vector_type(4)));
typedef unsigned u32x4 __attribute__((ext_vector_type(4)));
typedef unsigned u32x2 __attribute__((ext_vector_type(2)));
typedef unsigned long long u64;
#define LAS __attribute__((address_space(3)))
#define GAS __attribute__((address_space(1)))
#define DI __device__ __forceinline__
#define RLX_AGENT __ATOMIC_RELAXED, __HIP_MEMORY_SCOPE_AGENT

constexpr int SQ = 48, DM = 640, NH = 32, DH = 128, SK = 48;
constexpr int HD = NH * DH;

struct Ptrs {
  const unsigned short* x[2];
  const unsigned short* k[2];
  const unsigned short* v[2];
  const unsigned short* wq[2];
  const unsigned short* wo[2];
  unsigned short* out;
  unsigned char* ws;
};

constexpr int E_OFF = 0;
constexpr int A_OFF = 1024;
constexpr int F1_OFF = 4096;
constexpr int F2_OFF = F1_OFF + 128 * 128;
constexpr int CTRL_BYTES = 65536;
constexpr size_t OF_OFF = CTRL_BYTES;
constexpr size_t OF_BR = (size_t)48 * 128 * 1024;
constexpr size_t PART_OFF = OF_OFF + 2 * OF_BR;
constexpr unsigned SPIN_CAP = 1u << 18;
constexpr int LEADER = 0;

constexpr int STAGE = 40960;
constexpr int LDS_BYTES = 4 * STAGE;
constexpr int SPARE = 3 * STAGE;
constexpr int XWORD = 76 * 1024;
constexpr unsigned W_RING = 0x9871065u;
constexpr int O_RING = 2 * 16384;
constexpr int KR = 5;

DI bf16x4 tr_read(LAS unsigned char* p) { return __builtin_amdgcn_ds_read_tr16_b64_v4bf16((LAS bf16x4*)p); }
DI bf16x8 cat8(bf16x4 a, bf16x4 b) { return __builtin_shufflevector(a, b, 0, 1, 2, 3, 4, 5, 6, 7); }
DI bf16x8 pack8(f32x4 a, f32x4 b) { return cat8(__builtin_convertvector(a, bf16x4), __builtin_convertvector(b, bf16x4)); }
DI f32x4 mfma32(bf16x8 a, bf16x8 b, f32x4 c) { return __builtin_amdgcn_mfma_f32_16x16x32_bf16(a, b, c, 0, 0, 0); }
DI float xor16_max(float v) {
  auto r = __builtin_amdgcn_permlane16_swap(__float_as_uint(v), __float_as_uint(v), false, false);
  return fmaxf(__uint_as_float(r[0]), __uint_as_float(r[1]));
}
DI float xor32_max(float v) {
  auto r = __builtin_amdgcn_permlane32_swap(__float_as_uint(v), __float_as_uint(v), false, false);
  return fmaxf(__uint_as_float(r[0]), __uint_as_float(r[1]));
}
DI float xor16_add(float v) {
  auto r = __builtin_amdgcn_permlane16_swap(__float_as_uint(v), __float_as_uint(v), false, false);
  return __uint_as_float(r[0]) + __uint_as_float(r[1]);
}
DI float xor32_add(float v) {
  auto r = __builtin_amdgcn_permlane32_swap(__float_as_uint(v), __float_as_uint(v), false, false);
  return __uint_as_float(r[0]) + __uint_as_float(r[1]);
}
#define WAIT_V(n) asm volatile("s_waitcnt vmcnt(%0)" ::"i"(n) : "memory")
#define WAIT_L0() asm volatile("s_waitcnt lgkmcnt(0)" ::: "memory")
#define BAR() do { asm volatile("" ::: "memory"); __builtin_amdgcn_s_barrier(); asm volatile("" ::: "memory"); } while (0)
#define SCHED0() __builtin_amdgcn_sched_barrier(0)
#define PIN_S(x) asm volatile("" : "+s"(x))
#define PIN_V(x) asm volatile("" : "+v"(x))
#define G0(m, v, p, pol) asm volatile("s_mov_b32 m0, %2\n\ts_nop 0\n\tglobal_load_lds_dwordx4 %0, %1" pol : : "v"(v), "s"(p), "s"(m) : "memory")
#define G1(v, p, pol) asm volatile("s_add_u32 m0, m0, 0x400\n\ts_nop 0\n\tglobal_load_lds_dwordx4 %0, %1" pol : : "v"(v), "s"(p) : "memory", "scc")
#define BUMP(zero, one) asm volatile("s_mov_b64 exec, 1\n\tglobal_atomic_add %0, %1, %2\n\ts_mov_b64 exec, -1" : : "v"(zero), "v"(one), "s"(P.ws + E_OFF) : "memory")

__global__ __launch_bounds__(512, 2) void fused(Ptrs P) {
  extern __shared__ __attribute__((aligned(16))) unsigned char smem[];
  LAS unsigned char* L = (LAS unsigned char*)smem;
  const int tid = threadIdx.x, lane = tid & 63;
  const int wv = __builtin_amdgcn_readfirstlane(tid >> 6);
  const int w = wv & 3;
  const int bid = blockIdx.x, xcd = bid & 7, idx = bid >> 3;
  const int br = xcd >> 2, kq = xcd & 3, hl = idx >> 2, head = kq * 8 + hl, mg = idx & 3;
  const bool p2 = idx >= 2;
  const int i2 = p2 ? idx - 2 : 0;
  const int mt = i2 / 5, nt = i2 % 5;
  const int g = lane >> 4, r16 = lane & 15, q4 = r16 >> 2, p4 = lane & 3;
  const int rot2 = mt * 5 + nt * 3;

  if (__builtin_expect(wv >= 4, 1)) {
    const unsigned lbase = (unsigned)(uintptr_t)L;
    const int xslot = lane & 7, xrow = lane >> 3;
    const unsigned xv0 = xrow * (DM * 2) + 16 * (xslot ^ (xrow >> 1));
    const unsigned xv1 = xv0 ^ 64;
    const unsigned char* xs = (const unsigned char*)P.x[br] + (size_t)((mg * 4 + w) * SQ) * (DM * 2);
    const int wsub = lane >> 5, wrow = (lane >> 2) & 7, wslot = lane & 3;
    const unsigned wv0 = wrow * (HD * 2) + 16 * (4 * wsub + (wslot ^ (wrow >> 2)));
    const unsigned wv1 = wv0 ^ 32;
    const unsigned char* wsb = (const unsigned char*)P.wq[br] + (size_t)(16 * w) * (HD * 2) + head * 256;
    int c = hl + ((mg & 1) ? 5 : 0) + ((mg & 2) ? 3 : 0);
    c -= (c >= 10) ? 10 : 0;
    const unsigned char *pa = wsb + (size_t)c * 64 * (HD * 2), *pb = xs + c * 128;
    unsigned buf = lbase, ma = lbase + 24576 + 4096 * w, mb = lbase + 6144 * w;
    const unsigned a0 = wv0, a1 = wv0 + 128, a2 = wv1 + 8 * HD * 2, a3 = wv1 + 8 * HD * 2 + 128;
    const unsigned b0 = xv0, b1 = xv1 + 10240, b2 = xv0 + 20480, b3 = xv1 + 30720, b4 = xv0 + 40960, b5 = xv1 + 51200;
    auto issue_stage = [&]() {
      G0(ma, a0, pa, "");
      c = (c == 9) ? 0 : c + 1; PIN_S(c);
      G1(a1, pa, "");
      buf = (buf == lbase + 2 * STAGE) ? lbase : buf + STAGE; PIN_S(buf);
      G1(a2, pa, "");
      ma = buf + 24576 + 4096 * w; PIN_S(ma);
      G1(a3, pa, "");
      G0(mb, b0, pb, "");
      pa = wsb + (size_t)c * 64 * (HD * 2); PIN_S(pa);
      G1(b1, pb, "");
      mb = buf + 6144 * w; PIN_S(mb);
      G1(b2, pb, "");
      const unsigned char* pn = xs + c * 128; PIN_S(pn);
      G1(b3, pb, "");
      G1(b4, pb, "");
      G1(b5, pb, "");
      pb = pn;
    };
    issue_stage();
    unsigned kvK = (4 * w + g) * 256 + 16 * (r16 ^ (4 * w + g));
    unsigned kvV = (4 * w + g) * 256 + 16 * (r16 ^ ((g << 2) | ((w & 1) << 1)));
    const unsigned char* kg = (const unsigned char*)P.k[br] + (size_t)(mg * 4 * NH + head) * (SK * DH * 2);
    const unsigned char* vg = (const unsigned char*)P.v[br] + (size_t)(mg * 4 * NH + head) * (SK * DH * 2);
    constexpr size_t BSTR = (size_t)NH * SK * DH * 2;
    const unsigned lw = lbase + w * 1024;
    unsigned u0 = wrow * (DM * 2) + 16 * (4 * wsub + (wslot ^ ((wrow >> 2) << 1)));
    unsigned u1 = u0 + 128, u2 = u0 + 8 * (DM * 2), u3 = u0 + 8 * (DM * 2) + 128;
    const int Ma = mt * 8 + ((2 * w) & 4) + ((2 * w + kq) & 3), Mb = mt * 8 + ((2 * w) & 4) + ((2 * w + 1 + kq) & 3);
    unsigned o0 = Ma * 131072 + lane * 16, o1 = o0 + 1024, o2 = Mb * 131072 + lane * 16, o3 = o2 + 1024;
    unsigned fo = F1_OFF + ((br * 16 + ((lane & 8) ? Mb / 3 : Ma / 3)) * 4 + kq) * 128 + (lane & 7) * 4;
    unsigned lzero = 0, lone = 1;
    PIN_V(kvK); PIN_V(kvV); PIN_V(u0); PIN_V(u1); PIN_V(u2); PIN_V(u3); PIN_V(o0); PIN_V(o1); PIN_V(o2); PIN_V(o3);
    PIN_V(fo); PIN_V(lzero); PIN_V(lone);
    const int kb = 3 + ((idx + xcd) & 1);
    const bool lbump = (bid == LEADER) && (wv == 4);
#pragma unroll 1
    for (int i = 1; i < 10; ++i) {
      if (lbump && i >= 3) BUMP(lzero, lone);
      issue_stage();
      if (i == kb) {
#pragma unroll
        for (int t = 0; t < 8; ++t) G0(lw + SPARE + 4096 * t, kvK, kg + (t >> 1) * BSTR + (t & 1) * 4096, " nt");
        WAIT_V(18);
      } else if (i == KR + 3) {
#pragma unroll
        for (int t = 0; t < 4; ++t) G0(lw + SPARE + 4096 * t, kvK, kg + t * BSTR + 8192, " nt");
#pragma unroll
        for (int t = 0; t < 4; ++t) G0(lw + SPARE + 16384 + 4096 * t, kvV, vg + t * BSTR, " nt");
        WAIT_V(18);
      } else {
        WAIT_V(10);
      }
      BAR();
    }
#pragma unroll
    for (int t = 0; t < 8; ++t) G0(lw + STAGE + 4096 * t, kvV, vg + (t >> 1) * BSTR + 4096 + (t & 1) * 4096, " nt");
    WAIT_V(8);
    BAR();
    if (!p2) {
      BAR();
      WAIT_V(0);
      BAR();
      return;
    }
    const unsigned char* wog = (const unsigned char*)P.wo[br] + (size_t)(kq * 1024 + 16 * w) * (DM * 2) + nt * 256;
    int wj = rot2 & 15;
    unsigned wtab = W_RING;
    const unsigned char* pw = wog + wj * (64 * DM * 2);
    unsigned mw = lbase + (wtab & 15) * 16384 + 4096 * w;
    auto issue_w2 = [&]() {
      G0(mw, u0, pw, "");
      wj = (wj + 1) & 15; wtab = (wtab >> 4) | ((wtab & 15) << 24); PIN_S(wj); PIN_S(wtab);
      G1(u1, pw, "");
      mw = lbase + (wtab & 15) * 16384 + 4096 * w; PIN_S(mw);
      G1(u2, pw, "");
      const unsigned char* pn = wog + wj * (64 * DM * 2); PIN_S(pn);
      G1(u3, pw, "");
      pw = pn;
    };
    const unsigned char* og = P.ws + OF_OFF + br * OF_BR + (size_t)(kq * 32) * 1024;
    int oj = rot2 & 15;
    const unsigned char* po = og + oj * 2048;
    unsigned mo = lbase + O_RING + 4096 * w;
    auto issue_o = [&]() {
      G0(mo, o0, po, " sc1");
      oj = (oj + 1) & 15; PIN_S(oj);
      G1(o1, po, " sc1");
      mo = (mo >= lbase + O_RING + 32768) ? mo - 32768 : mo + 16384; PIN_S(mo);
      G1(o2, po, " sc1");
      const unsigned char* pn = og + oj * 2048; PIN_S(pn);
      G1(o3, po, " sc1");
      po = pn;
    };
    BAR();
    WAIT_V(0);
    issue_w2(); issue_w2();
    BAR();
    issue_w2();
    BAR();
#pragma unroll 1
    for (int j = 0; j < 4; ++j) issue_w2();
    {
      const unsigned X = __builtin_amdgcn_readfirstlane(*(volatile LAS unsigned*)(L + XWORD));
      GAS unsigned* fp = (GAS unsigned*)(P.ws + fo);
#pragma unroll 1
      for (unsigned spins = 0; spins < SPIN_CAP; ++spins) {
        const unsigned v = __hip_atomic_load(fp, RLX_AGENT);
        if (__all(v == X)) break;
        __builtin_amdgcn_s_sleep(4);
      }
    }
    issue_o(); issue_o();
    WAIT_V(4);
    BAR();
    issue_o();
    WAIT_V(4);
    BAR();
#pragma unroll 1
    for (int j = 0; j < 9; ++j) { issue_o(); issue_w2(); WAIT_V(8); BAR(); }
#pragma unroll 1
    for (int j = 9; j < 13; ++j) { issue_o(); WAIT_V(4); BAR(); }
    WAIT_V(0);
    BAR();
    return;
  }

  GAS unsigned* ctl = (GAS unsigned*)P.ws;
  GAS u64* ep = (GAS u64*)(P.ws + E_OFF);
  const bool leader = (bid == LEADER) && (w == 1);
  const bool member = (bid != LEADER) && (w == 0);
  u64 e0 = 0;
  unsigned Xl = 1;
  if (w < 2) e0 = __hip_atomic_load(ep, RLX_AGENT);
  const unsigned vzero = 0, vone = 1;
  const __amdgpu_buffer_rsrc_t rc = __builtin_amdgcn_make_buffer_rsrc((void*)P.ws, 0, CTRL_BYTES, 0x00020000);
  if (leader) {
    u32x4 f[9];
    f[0] = __builtin_amdgcn_raw_buffer_load_b128(rc, A_OFF + lane * 16, 0, 16);
#pragma unroll
    for (int i = 0; i < 4; ++i) f[1 + i] = __builtin_amdgcn_raw_buffer_load_b128(rc, F1_OFF + ((lane + 64 * i) >> 1) * 128 + (lane & 1) * 16, 0, 16);
#pragma unroll
    for (int i = 0; i < 4; ++i) f[5 + i] = __builtin_amdgcn_raw_buffer_load_b128(rc, F2_OFF + min(lane + 64 * i, 239) * 128, 0, 16);
    while (true) {
      bool hit = false;
#pragma unroll
      for (int i = 0; i < 9; ++i) hit |= (f[i][0] == Xl) | (f[i][1] == Xl) | (f[i][2] == Xl) | (f[i][3] == Xl);
      if (!__any(hit)) break;
      ++Xl;
    }
    if (lane == 0) __hip_atomic_store(ep, (u64)(~(unsigned)e0 & 0x80000000u) | ((u64)Xl << 32), RLX_AGENT);
  }

  f32x4 acc[3][8];
#pragma unroll
  for (int m = 0; m < 3; ++m)
#pragma unroll
    for (int c = 0; c < 8; ++c) acc[m][c] = f32x4{0.f, 0.f, 0.f, 0.f};

  const int xoff0 = w * 6144 + r16 * 128 + 16 * (g ^ ((r16 >> 1) & 7));
  const int woff00 = 24576 + 2048 * g + 64 * q4 + 16 * (2 * (g & 1) + (p4 >> 1)) + 8 * (p4 & 1);
  bf16x8 xf[2][3], wf[2][8];
  auto read_frags = [&](int bufo, int ks, bf16x8 (&xd)[3], bf16x8 (&wd)[8]) {
    LAS unsigned char* xb = L + bufo + (xoff0 ^ (64 * ks));
    LAS unsigned char* w00 = L + bufo + woff00 + 8192 * ks;
    LAS unsigned char* w01 = L + bufo + (woff00 ^ 16) + 256 + 8192 * ks;
    LAS unsigned char* w10 = L + bufo + (woff00 ^ 32) + 8192 * ks;
    LAS unsigned char* w11 = L + bufo + (woff00 ^ 48) + 256 + 8192 * ks;
#pragma unroll
    for (int m = 0; m < 3; ++m) xd[m] = *(LAS bf16x8*)(xb + m * 2048);
#pragma unroll
    for (int c = 0; c < 8; ++c) {
      bf16x4 lo = tr_read(((c & 1) ? w10 : w00) + 512 * (c >> 1));
      bf16x4 hi = tr_read(((c & 1) ? w11 : w01) + 512 * (c >> 1));
      wd[c] = cat8(lo, hi);
    }
  };
  auto mma = [&](bf16x8 (&xd)[3], bf16x8 (&wd)[8]) {
#pragma unroll
    for (int c = 0; c < 8; ++c)
#pragma unroll
      for (int m = 0; m < 3; ++m) acc[m][c] = mfma32(wd[c], xd[m], acc[m][c]);
  };
  auto interleave = [&](int nr, int tail) {
#pragma unroll
    for (int i = 0; i < nr; ++i) {
      __builtin_amdgcn_sched_group_barrier(0x008, 1, 0);
      __builtin_amdgcn_sched_group_barrier(0x100, 1, 0);
    }
#pragma unroll
    for (int i = 0; i < tail; ++i) __builtin_amdgcn_sched_group_barrier(0x008, 1, 0);
  };
  bf16x8 kf[3][4];
  auto read_k = [&](int st, int off) {
    LAS unsigned char* kb_ = L + off + r16 * 256 + 8 * (g & 1);
#pragma unroll
    for (int s = 0; s < 4; ++s) {
      bf16x4 lo = *(LAS bf16x4*)(kb_ + 16 * ((4 * s + (g >> 1)) ^ r16));
      bf16x4 hi = *(LAS bf16x4*)(kb_ + 16 * ((4 * s + 2 + (g >> 1)) ^ r16));
      kf[st][s] = cat8(lo, hi);
    }
  };
#pragma unroll
  for (int st = 0; st < 2; ++st)
#pragma unroll
    for (int s = 0; s < 4; ++s) kf[st][s] = __builtin_bit_cast(bf16x8, u32x4{0u, 0u, 0u, 0u});

  BAR();
  read_frags(0, 0, xf[0], wf[0]);
  SCHED0();
  read_frags(0, 1, xf[1], wf[1]);
  mma(xf[0], wf[0]);
  interleave(19, 5);
  SCHED0();
  WAIT_L0();
  WAIT_V(0);
  BAR();
  u64 e1 = e0;
  {
    int cur = STAGE;
#pragma unroll 1
    for (int s = 1; s < 10; ++s) {
      if (s == KR) {
        read_k(0, SPARE + 8192 * w); read_k(1, SPARE + 8192 * w + 4096);
        if (member) asm volatile("global_load_dwordx2 %0, %1, %2 sc1" : "+v"(e1) : "v"(vzero), "s"(P.ws + E_OFF) : "memory");
      }
      SCHED0();
      read_frags(cur, 0, xf[0], wf[0]);
      mma(xf[1], wf[1]);
      interleave(19, 5);
      SCHED0();
      read_frags(cur, 1, xf[1], wf[1]);
      mma(xf[0], wf[0]);
      interleave(19, 5);
      SCHED0();
      cur = (cur == 2 * STAGE) ? 0 : cur + STAGE;
      WAIT_L0();
      BAR();
    }
  }
  SCHED0();
  asm volatile("s_waitcnt vmcnt(0)" : "+v"(e1) : : "memory");
  read_k(2, SPARE + 4096 * w);
#pragma unroll
  for (int m = 0; m < 3; ++m)
#pragma unroll
    for (int c = 0; c < 8; ++c) acc[m][c] = mfma32(wf[1][c], xf[1][m], acc[m][c]);
  const bf16x4 zero4 = __builtin_bit_cast(bf16x4, u32x2{0u, 0u});
  auto qk = [&](int m, f32x4 (&sc)[3]) {
#pragma unroll
    for (int st = 0; st < 3; ++st) sc[st] = f32x4{0.f, 0.f, 0.f, 0.f};
#pragma unroll
    for (int s = 0; s < 4; ++s) {
      const bf16x8 qf = pack8(acc[m][2 * s], acc[m][2 * s + 1]);
#pragma unroll
      for (int st = 0; st < 3; ++st) sc[st] = mfma32(kf[st][s], qf, sc[st]);
    }
  };
  auto softmax = [&](f32x4 (&sc)[3], bf16x8& pf, bf16x8& pl) {
    const float cexp = 0.08838834764831845f * 1.4426950408889634f;
    float mx = sc[0][0];
#pragma unroll
    for (int st = 0; st < 3; ++st)
#pragma unroll
      for (int r = 0; r < 4; ++r) mx = fmaxf(mx, sc[st][r]);
    mx = xor16_max(mx);
    mx = xor32_max(mx);
    const float mb = mx * cexp;
#pragma unroll
    for (int st = 0; st < 3; ++st)
#pragma unroll
      for (int r = 0; r < 4; ++r) sc[st][r] = __builtin_amdgcn_exp2f(sc[st][r] * cexp - mb);
    const f32x4 s4 = (sc[0] + sc[1]) + sc[2];
    float sum = (s4[0] + s4[1]) + (s4[2] + s4[3]);
    sum = xor16_add(sum);
    sum = xor32_add(sum);
    const float inv = __builtin_amdgcn_rcpf(sum);
#pragma unroll
    for (int st = 0; st < 3; ++st) sc[st] *= inv;
    pf = pack8(sc[0], sc[1]);
    pl = cat8(__builtin_convertvector(sc[2], bf16x4), zero4);
  };
  f32x4 sc0[3], sc1[3], sc2[3];
  bf16x8 pf0, pl0, pf1, pl1, pf2, pl2;
  qk(0, sc0);
  softmax(sc0, pf0, pl0);
  asm volatile("" : "+v"(pf0), "+v"(pl0));
  if (member) {
#pragma unroll 1
    for (unsigned spins = 0; e1 == e0 && spins < SPIN_CAP; ++spins) {
      __builtin_amdgcn_s_sleep(2);
      e1 = __hip_atomic_load(ep, RLX_AGENT);
    }
    Xl = (unsigned)(e1 >> 32);
    if (lane == 0) __hip_atomic_store(ctl + A_OFF / 4 + bid, Xl, RLX_AGENT);
  }
  if ((member || leader) && lane == 0) *(volatile LAS unsigned*)(L + XWORD) = Xl;
  WAIT_L0();
  BAR();
  const unsigned X = __builtin_amdgcn_readfirstlane(*(volatile LAS unsigned*)(L + XWORD));
  const __amdgpu_buffer_rsrc_t ro = __builtin_amdgcn_make_buffer_rsrc((void*)(P.ws + OF_OFF + br * OF_BR), 0, (int)OF_BR, 0x00020000);
  {
    const int vsw = (q4 << 2) | ((g & 1) << 1);
    LAS unsigned char* V0 = L + SPARE + 16384 + 4096 * w;
    LAS unsigned char* V1 = L + STAGE + 8192 * w;
    bf16x8 v01[8], v2z[8];
#pragma unroll
    for (int c = 0; c < 8; ++c) {
      const int a = (4 * g + q4) * 256 + 16 * ((2 * c + (p4 >> 1)) ^ vsw) + 8 * (p4 & 1);
      v01[c] = cat8(tr_read(V0 + a), tr_read(V1 + a));
      v2z[c] = cat8(tr_read(V1 + 4096 + a), zero4);
    }
    qk(1, sc1);
    const int obase = (((mg * 4 + w) * 3) * 128 + head * 4) * 1024 + lane * 16;
    auto pv = [&](int m, bf16x8 pf, bf16x8 pl) {
#pragma unroll
      for (int s = 0; s < 4; ++s) {
        f32x4 o0 = mfma32(v01[2 * s], pf, f32x4{0.f, 0.f, 0.f, 0.f});
        f32x4 o1 = mfma32(v01[2 * s + 1], pf, f32x4{0.f, 0.f, 0.f, 0.f});
        o0 = mfma32(v2z[2 * s], pl, o0);
        o1 = mfma32(v2z[2 * s + 1], pl, o1);
        __builtin_amdgcn_raw_buffer_store_b128(__builtin_bit_cast(u32x4, pack8(o0, o1)), ro, s * 1024 + obase + m * 131072, 0, 16);
      }
    };
    auto spread = [&](int nm, int nv) {
#pragma unroll
      for (int i = 0; i < 4; ++i) {
        if (nm == 4) __builtin_amdgcn_sched_group_barrier(0x008, 4, 0); else __builtin_amdgcn_sched_group_barrier(0x008, 7, 0);
        if (nv == 24) __builtin_amdgcn_sched_group_barrier(0x002, 24, 0); else __builtin_amdgcn_sched_group_barrier(0x002, 20, 0);
        __builtin_amdgcn_sched_group_barrier(0x040, 1, 0);
      }
    };
    SCHED0();
    pv(0, pf0, pl0);
    softmax(sc1, pf1, pl1);
    qk(2, sc2);
    spread(7, 24);
    SCHED0();
    pv(1, pf1, pl1);
    softmax(sc2, pf2, pl2);
    spread(4, 20);
    SCHED0();
    pv(2, pf2, pl2);
  }
  WAIT_V(0);
  if (lane == 0) __hip_atomic_store(ctl + (F1_OFF + ((br * 16 + mg * 4 + w) * 4 + kq) * 128) / 4 + hl, X, RLX_AGENT);
  if (!p2) {
    if (leader) {
#pragma unroll 1
      for (unsigned spins = 0; spins < SPIN_CAP; ++spins) {
        BUMP(vzero, vone);
        const u32x4 la = __builtin_amdgcn_raw_buffer_load_b128(rc, A_OFF + lane * 16, 0, 16);
        if (__all(((la[0] == X) | (lane == 0)) & (la[1] == X) & (la[2] == X) & (la[3] == X))) break;
        __builtin_amdgcn_s_sleep(8);
      }
    }
    return;
  }
  WAIT_L0();
  BAR();

  const int wm = w >> 1, wn = w & 1;
  f32x4 ac2[4][4];
#pragma unroll
  for (int m = 0; m < 4; ++m)
#pragma unroll
    for (int c = 0; c < 4; ++c) ac2[m][c] = f32x4{0.f, 0.f, 0.f, 0.f};
  const int ooff = O_RING + wm * 8192 + lane * 16;
  const int w2o = 2048 * (g >> 1) + 1024 * wn + 64 * (4 * (g & 1) + q4) + 16 * (2 * (g & 1) + (p4 >> 1)) + 8 * (p4 & 1);
  bf16x8 of[2][4], wg[2][4];
  auto read2 = [&](int oslot, int wslot_, int ks, bf16x8 (&od)[4], bf16x8 (&wd)[4]) {
    LAS unsigned char* ob = L + oslot + ooff + 1024 * ks;
    LAS unsigned char* b0 = L + wslot_ + w2o + 8192 * ks;
    LAS unsigned char* b1 = L + wslot_ + (w2o ^ 32) + 8192 * ks;
#pragma unroll
    for (int m = 0; m < 4; ++m) od[m] = *(LAS bf16x8*)(ob + m * 2048);
#pragma unroll
    for (int c = 0; c < 4; ++c) {
      LAS unsigned char* bp = ((c & 1) ? b1 : b0) + 512 * (c >> 1);
      wd[c] = cat8(tr_read(bp), tr_read(bp + 4096));
    }
  };
  auto mma2 = [&](bf16x8 (&od)[4], bf16x8 (&wd)[4]) {
#pragma unroll
    for (int c = 0; c < 4; ++c)
#pragma unroll
      for (int m = 0; m < 4; ++m) ac2[m][c] = mfma32(wd[c], od[m], ac2[m][c]);
  };
  BAR();
  read2(0, (W_RING & 15) * 16384, 0, of[0], wg[0]);
  {
    int os = 0, wsl = (W_RING & 15) * 16384;
    unsigned tab = (W_RING >> 4) | ((W_RING & 15) << 24);
#pragma unroll 1
    for (int j = 0; j < 16; ++j) {
      SCHED0();
      read2(os, wsl, 1, of[1], wg[1]);
      mma2(of[0], wg[0]);
      os = (os == 32768) ? 0 : os + 16384;
      wsl = (int)(tab & 15) * 16384;
      tab = (tab >> 4) | ((tab & 15) << 24);
      interleave(12, 4);
      SCHED0();
      WAIT_L0();
      BAR();
      SCHED0();
      read2(os, wsl, 0, of[0], wg[0]);
      mma2(of[1], wg[1]);
      interleave(12, 4);
    }
  }
  SCHED0();
  const int tile = br * 30 + i2;
  const __amdgpu_buffer_rsrc_t rs = __builtin_amdgcn_make_buffer_rsrc((void*)(P.ws + PART_OFF + (size_t)tile * (4 * 4 * 8 * 1024)), 0, 4 * 4 * 8 * 1024, 0x00020000);
#pragma unroll
  for (int j = 1; j < 4; ++j)
#pragma unroll
    for (int c = 0; c < 2; ++c)
      __builtin_amdgcn_raw_buffer_store_b128(__builtin_bit_cast(u32x4, pack8(ac2[j][2 * c], ac2[j][2 * c + 1])), rs, lane * 16 + (j * 2 + c) * 1024 + (kq * 4 + w) * 8192, 0, 16);
  WAIT_V(0);
  GAS unsigned* f2 = ctl + (F2_OFF + (tile * 4 + w) * 128) / 4;
  if (lane == 0) __hip_atomic_store(f2 + kq, X, RLX_AGENT);
#pragma unroll 1
  for (unsigned spins = 0; spins < SPIN_CAP; ++spins) {
    const unsigned v = __hip_atomic_load(f2 + (lane & 3), RLX_AGENT);
    if (__all(v == X)) break;
    __builtin_amdgcn_s_sleep(1);
  }
  __builtin_amdgcn_fence(__ATOMIC_ACQUIRE, "wavefront");
  u32x4 pr[3][2];
#pragma unroll
  for (int j = 1; j < 4; ++j) {
    const int src = (kq - j) & 3;
#pragma unroll
    for (int c = 0; c < 2; ++c)
      pr[j - 1][c] = __builtin_amdgcn_raw_buffer_load_b128(rs, lane * 16 + (j * 2 + c) * 1024, (src * 4 + w) * 8192, 16);
  }
  unsigned short* orow = P.out + (size_t)(mt * 128 + wm * 64 + kq * 16 + r16) * 1280 + br * 640 + nt * 128 + wn * 64 + 4 * g;
#pragma unroll
  for (int c = 0; c < 4; ++c) {
    f32x4 s = ac2[0][c];
#pragma unroll
    for (int j = 0; j < 3; ++j) {
      const unsigned lo = pr[j][c >> 1][2 * (c & 1)], hi = pr[j][c >> 1][2 * (c & 1) + 1];
      s += __builtin_bit_cast(f32x4, u32x4{lo << 16, lo & 0xffff0000u, hi << 16, hi & 0xffff0000u});
    }
    *(GAS bf16x4*)(orow + 16 * c) = __builtin_convertvector(s, bf16x4);
  }
}

extern "C" void kernel_launch(void* const* d_in, const int* in_sizes, int n_in, void* d_out, int out_size,
                              void* d_ws, size_t ws_size, hipStream_t stream) {
  Ptrs P;
  for (int i = 0; i < 2; ++i) {
    P.x[i] = (const unsigned short*)d_in[5 * i + 0];
    P.k[i] = (const unsigned short*)d_in[5 * i + 1];
    P.v[i] = (const unsigned short*)d_in[5 * i + 2];
    P.wq[i] = (const unsigned short*)d_in[5 * i + 3];
    P.wo[i] = (const unsigned short*)d_in[5 * i + 4];
  }
  P.out = (unsigned short*)d_out;
  P.ws = (unsigned char*)d_ws;
  (void)hipFuncSetAttribute((const void*)fused, hipFuncAttributeMaxDynamicSharedMemorySize, LDS_BYTES);
  fused<<<256, 512, LDS_BYTES, stream>>>(P);
}
```

```cpp
#include <hip/hip_runtime.h>
#include <hip/hip_bf16.h>

typedef __bf16 bf16x8 __attribute__((ext_vector_type(8)));
typedef __bf16 bf16x4 __attribute__((ext_vector_type(4)));
typedef float f32x4 __attribute__((ext_vector_type(4)));
typedef unsigned u32x4 __attribute__((ext_vector_type(4)));
typedef unsigned u32x2 __attribute__((ext_vector_type(2)));
typedef unsigned long long u64;
#define LAS __attribute__((address_space(3)))
#define GAS __attribute__((address_space(1)))
#define DI __device__ __forceinline__
#define RLX_AGENT __ATOMIC_RELAXED, __HIP_MEMORY_SCOPE_AGENT

constexpr int SQ = 48, DM = 640, NH = 32, DH = 128, SK = 48;
constexpr int HD = NH * DH;

struct Ptrs {
  const unsigned short* x[2];
  const unsigned short* k[2];
  const unsigned short* v[2];
  const unsigned short* wq[2];
  const unsigned short* wo[2];
  unsigned short* out;
  unsigned char* ws;
};

constexpr int E_OFF = 0;
constexpr int A_OFF = 1024;
constexpr int F1_OFF = 4096;
constexpr int F2_OFF = F1_OFF + 128 * 128;
constexpr int CTRL_BYTES = 65536;
constexpr size_t OF_OFF = CTRL_BYTES;
constexpr int MS = 131072, CS = 2048;
constexpr size_t OF_BR = (size_t)48 * MS;
constexpr size_t PART_OFF = OF_OFF + 2 * OF_BR;
constexpr unsigned SPIN_CAP = 1u << 18;
constexpr int IDLE2 = 28;
constexpr int LEADER = IDLE2 * 8;

constexpr int STAGE = 40960;
constexpr int LDS_BYTES = 4 * STAGE;
constexpr int SPARE = 3 * STAGE;
constexpr int XWORD = 152 * 1024;
constexpr unsigned W_RING = 0x9821043u;
constexpr int O_RING = 5 * 16384;
constexpr int V12 = 2 * STAGE;
constexpr int PS = 4;
constexpr int HS = 6;

DI bf16x4 tr_read(LAS unsigned char* p) { return __builtin_amdgcn_ds_read_tr16_b64_v4bf16((LAS bf16x4*)p); }
DI bf16x8 cat8(bf16x4 a, bf16x4 b) { return __builtin_shufflevector(a, b, 0, 1, 2, 3, 4, 5, 6, 7); }
DI bf16x8 pack8(f32x4 a, f32x4 b) { return cat8(__builtin_convertvector(a, bf16x4), __builtin_convertvector(b, bf16x4)); }
DI f32x4 mfma32(bf16x8 a, bf16x8 b, f32x4 c) { return __builtin_amdgcn_mfma_f32_16x16x32_bf16(a, b, c, 0, 0, 0); }
#define WAIT_V(n) asm volatile("s_waitcnt vmcnt(%0)" ::"i"(n) : "memory")
#define WAIT_L0() asm volatile("s_waitcnt lgkmcnt(0)" ::: "memory")
#define BAR() do { asm volatile("" ::: "memory"); __builtin_amdgcn_s_barrier(); asm volatile("" ::: "memory"); } while (0)
#define SCHED0() __builtin_amdgcn_sched_barrier(0)
#define PIN_S(x) asm volatile("" : "+s"(x))
#define PIN_V(x) asm volatile("" : "+v"(x))
#define G0(m, v, p, pol) asm volatile("s_mov_b32 m0, %2\n\ts_nop 0\n\tglobal_load_lds_dwordx4 %0, %1" pol : : "v"(v), "s"(p), "s"(m) : "memory")
#define G1(v, p, pol) asm volatile("s_add_u32 m0, m0, 0x400\n\ts_nop 0\n\tglobal_load_lds_dwordx4 %0, %1" pol : : "v"(v), "s"(p) : "memory", "scc")
#define BUMP(zero, one) asm volatile("s_mov_b64 exec, 1\n\tglobal_atomic_add %0, %1, %2\n\ts_mov_b64 exec, -1" : : "v"(zero), "v"(one), "s"(P.ws + E_OFF) : "memory")

__global__ __launch_bounds__(512, 2) void fused(Ptrs P) {
  extern __shared__ __attribute__((aligned(16))) unsigned char smem[];
  LAS unsigned char* L = (LAS unsigned char*)smem;
  const unsigned char *px, *pwq;
  asm volatile("s_load_dwordx2 %0, %2, %3 offset:0x0\n\ts_load_dwordx2 %1, %2, %3 offset:0x30"
               : "=&s"(px), "=&s"(pwq) : "s"(__builtin_amdgcn_kernarg_segment_ptr()), "s"((blockIdx.x & 4) * 2));
  unsigned junk;
  {
    const unsigned char* pc;
    unsigned len;
    asm volatile("s_getpc_b64 %0\n.Lcode_top:\n\ts_mov_b32 %1, .Lcode_to-.Lcode_top" : "=s"(pc), "=s"(len));
    const unsigned off = min((threadIdx.x & 0x1c0) + (blockIdx.x >> 3) * 512, len);
    asm volatile("global_load_dword %0, %1, %2" : "=v"(junk) : "v"(off), "s"(pc) : "memory");
  }
  SCHED0();
  const int tid = threadIdx.x, lane = tid & 63;
  const int wv = __builtin_amdgcn_readfirstlane(tid >> 6);
  const int w = wv & 3;
  const int bid = blockIdx.x, xcd = bid & 7, idx = bid >> 3;
  const int br = xcd >> 2, kq = xcd & 3, hl = idx >> 2, head = kq * 8 + hl, mg = idx & 3;
  const bool p2 = (idx >> 1) != IDLE2 / 2;
  const int i2 = idx < IDLE2 ? idx : idx - 2;
  const int mt = i2 / 5, nt = i2 % 5;
  const int g = lane >> 4, r16 = lane & 15, q4 = r16 >> 2, p4 = lane & 3;
  const int rot2 = (0xda8520 >> (4 * mt)) & 15;

  if (__builtin_expect(wv >= 4, 1)) {
    const unsigned lbase = (unsigned)(uintptr_t)L;
    const int xslot = lane & 7, xrow = lane >> 3;
    const unsigned xv0 = xrow * (DM * 2) + 16 * (xslot ^ (xrow >> 1));
    const unsigned xv1 = xv0 ^ 64;
    const int wsub = lane >> 5, wrow = (lane >> 2) & 7, wslot = lane & 3;
    const unsigned wv0 = wrow * (HD * 2) + 16 * (4 * wsub + (wslot ^ (wrow >> 2)));
    const unsigned wv1 = wv0 ^ 32;
    int c = (hl >> 1) + ((0x5140 >> (4 * mg)) & 15) + ((0x7250 >> (4 * kq)) & 15);
    c -= (c >= 10) ? 10 : 0;
    unsigned buf = lbase, ma = lbase + 24576 + 4096 * w, mb = lbase + 6144 * w;
    const unsigned wo_ = 16 * w * (HD * 2) + head * 256, xo_ = (mg * 4 + w) * SQ * (DM * 2);
    unsigned a0 = wv0 + wo_, a1 = wv0 + wo_ + 128, a2 = wv1 + wo_ + 8 * HD * 2, a3 = wv1 + wo_ + 8 * HD * 2 + 128;
    unsigned b0 = xv0 + xo_, b1 = xv1 + xo_ + 10240, b2 = xv0 + xo_ + 20480, b3 = xv1 + xo_ + 30720, b4 = xv0 + xo_ + 40960, b5 = xv1 + xo_ + 51200;
    unsigned ca = c * 64 * (HD * 2), cb = c * 128;
    PIN_V(a0); PIN_V(a1); PIN_V(a2); PIN_V(a3); PIN_V(b0); PIN_V(b1); PIN_V(b2); PIN_V(b3); PIN_V(b4); PIN_V(b5);
    PIN_S(ca); PIN_S(cb); PIN_S(ma); PIN_S(mb);
    asm volatile("s_waitcnt lgkmcnt(0)" : "+s"(px), "+s"(pwq));
    const unsigned char *xs = px, *wsb = pwq;
    const unsigned char *pa = wsb + ca, *pb = xs + cb;
    auto issue_stage = [&]() {
      G0(ma, a0, pa, "");
      c = (c == 9) ? 0 : c + 1; PIN_S(c);
      G1(a1, pa, "");
      buf = (buf == lbase) ? lbase + 2 * STAGE : buf - STAGE; PIN_S(buf);
      G1(a2, pa, "");
      ma = buf + 24576 + 4096 * w; PIN_S(ma);
      G1(a3, pa, "");
      G0(mb, b0, pb, "");
      pa = wsb + (size_t)c * 64 * (HD * 2); PIN_S(pa);
      G1(b1, pb, "");
      mb = buf + 6144 * w; PIN_S(mb);
      G1(b2, pb, "");
      const unsigned char* pn = xs + c * 128; PIN_S(pn);
      G1(b3, pb, "");
      G1(b4, pb, "");
      G1(b5, pb, "");
      pb = pn;
    };
    issue_stage();
    unsigned kvK = (4 * w + g) * 256 + 16 * (r16 ^ (4 * w + g));
    unsigned kvV = (4 * w + g) * 256 + 16 * (r16 ^ ((g << 2) | ((w & 1) << 1)));
    const unsigned char* kg = (const unsigned char*)P.k[br] + (size_t)(mg * 4 * NH + head) * (SK * DH * 2);
    const unsigned char* vg = (const unsigned char*)P.v[br] + (size_t)(mg * 4 * NH + head) * (SK * DH * 2);
    constexpr size_t BSTR = (size_t)NH * SK * DH * 2;
    const unsigned lw = lbase + w * 1024;
    unsigned u0 = wrow * (DM * 2) + 16 * (4 * wsub + (wslot ^ ((wrow >> 2) << 1)));
    unsigned u1 = u0 + 128, u2 = u0 + 8 * (DM * 2), u3 = u0 + 8 * (DM * 2) + 128;
    const int Ma = mt * 8 + ((2 * w) & 4) + ((2 * w + kq) & 3), Mb = mt * 8 + ((2 * w) & 4) + ((2 * w + 1 + kq) & 3);
    unsigned o0 = Ma * MS + lane * 16, o1 = o0 + 1024, o2 = Mb * MS + lane * 16, o3 = o2 + 1024;
    unsigned fo = F1_OFF + ((br * 16 + ((lane & 8) ? Mb / 3 : Ma / 3)) * 4 + kq) * 128 + (lane & 7) * 4;
    unsigned lzero = 0, lone = 1;
    PIN_V(kvK); PIN_V(kvV); PIN_V(u0); PIN_V(u1); PIN_V(u2); PIN_V(u3); PIN_V(o0); PIN_V(o1); PIN_V(o2); PIN_V(o3);
    PIN_V(fo); PIN_V(lzero); PIN_V(lone);
    const int kb = 3 + ((idx + xcd) & 1);
    const bool lbump = (bid == LEADER) && (wv == 4);
#pragma unroll 1
    for (int i = 1; i < 10; ++i) {
      if (lbump && i >= 3) BUMP(lzero, lone);
      issue_stage();
      if (i == kb) {
#pragma unroll
        for (int t = 0; t < 8; ++t) G0(lw + SPARE + 4096 * (t >> 1) + 16384 * (t & 1), kvK, kg + (t >> 1) * BSTR + (t & 1) * 4096, " nt");
        WAIT_V(18);
      } else if (i == PS + 3) {
#pragma unroll
        for (int t = 0; t < 4; ++t) G0(lw + SPARE + 4096 * t, kvK, kg + t * BSTR + 8192, " nt");
#pragma unroll
        for (int t = 0; t < 4; ++t) G0(lw + SPARE + 16384 + 4096 * t, kvV, vg + t * BSTR, " nt");
        WAIT_V(18);
      } else {
        WAIT_V(10);
      }
      BAR();
    }
    asm volatile("" : : "v"(junk));
#pragma unroll
    for (int t = 0; t < 8; ++t) G0(lw + V12 + 4096 * (t >> 1) + 16384 * (t & 1), kvV, vg + (t >> 1) * BSTR + 4096 + (t & 1) * 4096, " nt");
    WAIT_V(8);
    BAR();
    if (!p2) {
      WAIT_V(0);
      BAR();
      return;
    }
    const unsigned char* wog = (const unsigned char*)P.wo[br] + (size_t)(kq * 1024 + 16 * w) * (DM * 2) + nt * 256;
    int wj = rot2 & 15;
    unsigned wtab = W_RING;
    const unsigned char* pw = wog + wj * (64 * DM * 2);
    unsigned mw = lbase + (wtab & 15) * 16384 + 4096 * w;
    auto issue_w2 = [&]() {
      G0(mw, u0, pw, "");
      wj = (wj + 1) & 15; wtab = (wtab >> 4) | ((wtab & 15) << 24); PIN_S(wj); PIN_S(wtab);
      G1(u1, pw, "");
      mw = lbase + (wtab & 15) * 16384 + 4096 * w; PIN_S(mw);
      G1(u2, pw, "");
      const unsigned char* pn = wog + wj * (64 * DM * 2); PIN_S(pn);
      G1(u3, pw, "");
      pw = pn;
    };
    const unsigned char* og = P.ws + OF_OFF + br * OF_BR + (size_t)(kq * 16) * CS;
    int oj = rot2 & 15;
    const unsigned char* po = og + oj * CS;
    unsigned mo = lbase + O_RING + 4096 * w;
    auto issue_o = [&]() {
      G0(mo, o0, po, " sc1");
      oj = (oj + 1) & 15; PIN_S(oj);
      G1(o1, po, " sc1");
      mo = (mo >= lbase + O_RING + 32768) ? mo - 32768 : mo + 16384; PIN_S(mo);
      G1(o2, po, " sc1");
      const unsigned char* pn = og + oj * CS; PIN_S(pn);
      G1(o3, po, " sc1");
      po = pn;
    };
    issue_w2(); issue_w2();
    WAIT_V(8);
    BAR();
    const unsigned X = __builtin_amdgcn_readfirstlane(*(volatile LAS unsigned*)(L + XWORD));
    WAIT_L0();
    WAIT_V(4);
    BAR();
#pragma unroll 1
    for (int j = 2; j < 7; ++j) issue_w2();
    unsigned rdy = 0, fl = ~X;
    const unsigned char* fpp = P.ws + fo;
    PIN_V(fpp);
    auto ask = [&]() { asm volatile("global_load_dword %0, %1, off sc1" : "+v"(fl) : "v"(fpp) : "memory"); };
    auto look = [&]() {
      asm volatile("" : "+v"(fl) : : "memory");
      const unsigned b = (unsigned)__builtin_amdgcn_ballot_w64(fl == X);
      rdy = b & (b >> 8) & 0xffu;
    };
    auto need = [&]() {
      if (__builtin_expect(!((rdy >> (oj >> 1)) & 1), 0)) {
#pragma unroll 1
        for (unsigned spins = 0; spins < SPIN_CAP; ++spins) {
          ask();
          WAIT_V(0);
          look();
          if ((rdy >> (oj >> 1)) & 1) break;
          __builtin_amdgcn_s_sleep(4);
        }
      }
    };
    auto refresh = [&]() { if (rdy != 0xffu) { look(); ask(); } };
    need(); issue_o(); need(); issue_o();
    WAIT_V(4);
    refresh();
    BAR();
    need(); issue_o();
    WAIT_V(4);
    refresh();
    BAR();
#pragma unroll 1
    for (int j = 0; j < 9; ++j) { need(); issue_o(); issue_w2(); WAIT_V(8); refresh(); BAR(); }
#pragma unroll 1
    for (int j = 9; j < 13; ++j) { need(); issue_o(); WAIT_V(4); refresh(); BAR(); }
    WAIT_V(0);
    BAR();
    return;
  }

  asm volatile("s_waitcnt lgkmcnt(0)" : "+s"(px), "+s"(pwq));
  GAS unsigned* ctl = (GAS unsigned*)P.ws;
  GAS u64* ep = (GAS u64*)(P.ws + E_OFF);
  const bool leader = (bid == LEADER) && (w == 1);
  const bool member = (bid != LEADER) && (w == 0);
  u64 e0 = 0;
  unsigned Xl = 1;
  if (w < 2) e0 = __hip_atomic_load(ep, RLX_AGENT);
  const unsigned vzero = 0, vone = 1;
  const __amdgpu_buffer_rsrc_t rc = __builtin_amdgcn_make_buffer_rsrc((void*)P.ws, 0, CTRL_BYTES, 0x00020000);
  if (__builtin_expect(leader, 0)) {
    u32x4 f[9];
    f[0] = __builtin_amdgcn_raw_buffer_load_b128(rc, A_OFF + lane * 16, 0, 16);
#pragma unroll
    for (int i = 0; i < 4; ++i) f[1 + i] = __builtin_amdgcn_raw_buffer_load_b128(rc, F1_OFF + ((lane + 64 * i) >> 1) * 128 + (lane & 1) * 16, 0, 16);
#pragma unroll
    for (int i = 0; i < 4; ++i) f[5 + i] = __builtin_amdgcn_raw_buffer_load_b128(rc, F2_OFF + min(lane + 64 * i, 239) * 128, 0, 16);
    while (true) {
      unsigned d = ~0u;
#pragma unroll
      for (int i = 0; i < 9; ++i) { d = min(min(d, f[i][0] ^ Xl), min(f[i][1] ^ Xl, min(f[i][2] ^ Xl, f[i][3] ^ Xl))); PIN_V(d); }
      if (!__any(d == 0)) break;
      ++Xl;
    }
    if (lane == 0) __hip_atomic_store(ep, (u64)(~(unsigned)e0 & 0x80000000u) | ((u64)Xl << 32), RLX_AGENT);
  }

  f32x4 acc[3][8];
#pragma unroll
  for (int m = 0; m < 3; ++m)
#pragma unroll
    for (int c = 0; c < 8; ++c) acc[m][c] = f32x4{0.f, 0.f, 0.f, 0.f};

  const int xoff0 = w * 6144 + r16 * 128 + 16 * (g ^ ((r16 >> 1) & 7));
  const int woff00 = 24576 + 2048 * g + 64 * q4 + 16 * (2 * (g & 1) + (p4 >> 1)) + 8 * (p4 & 1);
  bf16x8 xf[2][3], wf[2][8];
  LAS unsigned char *xb0 = L + xoff0, *xb1 = L + (xoff0 ^ 64);
  LAS unsigned char *w00 = L + woff00, *w01 = L + (woff00 ^ 16) + 256, *w10 = L + (woff00 ^ 32), *w11 = L + (woff00 ^ 48) + 256;
  auto read_frags = [&](int ks, bf16x8 (&xd)[3], bf16x8 (&wd)[8]) {
#pragma unroll
    for (int m = 0; m < 3; ++m) xd[m] = *(LAS bf16x8*)((ks ? xb1 : xb0) + m * 2048);
#pragma unroll
    for (int c = 0; c < 8; ++c) {
      bf16x4 lo = tr_read(((c & 1) ? w10 : w00) + 8192 * ks + 512 * (c >> 1));
      bf16x4 hi = tr_read(((c & 1) ? w11 : w01) + 8192 * ks + 512 * (c >> 1));
      wd[c] = cat8(lo, hi);
    }
  };
  auto step = [&](int d) {
    xb0 += d; xb1 += d; w00 += d; w01 += d; w10 += d; w11 += d;
#pragma unroll
    for (int i = 0; i < 19; ++i) {
      __builtin_amdgcn_sched_group_barrier(0x008, 1, 0);
      __builtin_amdgcn_sched_group_barrier(0x100, 1, 0);
    }
#pragma unroll
    for (int i = 0; i < 3; ++i) {
      __builtin_amdgcn_sched_group_barrier(0x008, 1, 0);
      __builtin_amdgcn_sched_group_barrier(0x002, 2, 0);
    }
    __builtin_amdgcn_sched_group_barrier(0x008, 2, 0);
    SCHED0();
    PIN_V(xb0); PIN_V(xb1); PIN_V(w00); PIN_V(w01); PIN_V(w10); PIN_V(w11);
  };
  auto mma = [&](bf16x8 (&xd)[3], bf16x8 (&wd)[8]) {
#pragma unroll
    for (int c = 0; c < 8; ++c)
#pragma unroll
      for (int m = 0; m < 3; ++m) acc[m][c] = mfma32(wd[c], xd[m], acc[m][c]);
  };
  auto interleave = [&](int nr, int tail) {
#pragma unroll
    for (int i = 0; i < nr; ++i) {
      __builtin_amdgcn_sched_group_barrier(0x008, 1, 0);
      __builtin_amdgcn_sched_group_barrier(0x100, 1, 0);
    }
#pragma unroll
    for (int i = 0; i < tail; ++i) __builtin_amdgcn_sched_group_barrier(0x008, 1, 0);
  };
  bf16x8 kf[3][4];
  LAS unsigned char* ka[8];
#pragma unroll
  for (int i = 0; i < 8; ++i) { ka[i] = L + SPARE + 4096 * w + r16 * 256 + 8 * (g & 1) + 16 * ((2 * i + (g >> 1)) ^ r16); PIN_V(ka[i]); }
  auto read_k1 = [&](int st, int s) { kf[st][s] = cat8(*(LAS bf16x4*)(ka[2 * s] + (st & 1) * 16384), *(LAS bf16x4*)(ka[2 * s + 1] + (st & 1) * 16384)); };
  auto read_k = [&](int st) {
#pragma unroll
    for (int s = 0; s < 4; ++s) read_k1(st, s);
  };
#pragma unroll
  for (int st = 0; st < 2; ++st)
#pragma unroll
    for (int s = 0; s < 4; ++s) asm volatile("" : "=v"(kf[st][s]));

  unsigned char* obase = P.ws + OF_OFF + br * OF_BR + (size_t)((mg * 4 + w) * 3 * MS + head * 2 * CS);
  GAS unsigned* f1 = ctl + (F1_OFF + ((br * 16 + mg * 4 + w) * 4 + kq) * 128) / 4 + hl;
  int l16 = lane * 16;
  int va0 = (int)(uintptr_t)L + V12 + (4 * g + q4) * 256 + 16 * ((p4 >> 1) ^ ((q4 << 2) | ((g & 1) << 1))) + 8 * (p4 & 1) + 4096 * w;
  PIN_S(obase); PIN_S(f1); PIN_V(l16); PIN_V(va0);
  BAR();
  read_frags(0, xf[0], wf[0]);
  SCHED0();
  read_frags(1, xf[1], wf[1]);
  mma(xf[0], wf[0]);
  step(2 * STAGE);
  if (leader) WAIT_V(0);
  u64 e1;
  asm volatile("" : "=v"(e1), "+v"(e0));
  {
    unsigned ev = 2u | (1u << PS) | ((member || leader) ? (1u << HS) : 0u);
    PIN_S(ev);
#pragma unroll 1
    for (int s = 1; s < 10; ++s) {
      __builtin_amdgcn_s_waitcnt(0xC07F);
      BAR();
      SCHED0();
      read_frags(0, xf[0], wf[0]);
      mma(xf[1], wf[1]);
      interleave(19, 5);
      SCHED0();
      read_frags(1, xf[1], wf[1]);
      mma(xf[0], wf[0]);
      step(((0x248 >> s) & 1) ? 2 * STAGE : -STAGE);
      if (__builtin_expect((ev >> s) & 1, 0)) {
        int sv = s;
        PIN_S(sv);
        if (sv == 1) __builtin_amdgcn_s_sleep(24);
        PIN_S(sv);
        if (sv == PS) {
          read_k(0); asm volatile("" ::: "memory"); read_k(1);
          if (member) asm volatile("global_load_dwordx2 %0, %1, %2 sc1" : "+v"(e1) : "v"(vzero), "s"(P.ws + E_OFF) : "memory");
        }
        PIN_S(sv);
        if (sv == HS) {
          unsigned xw = Xl;
          if (member) {
            u64 e2;
            asm volatile("s_waitcnt vmcnt(0)\n\tv_mov_b64 %0, %1" : "=v"(e2) : "v"(e1) : "memory");
#pragma unroll 1
            for (unsigned spins = 0; e2 == e0 && spins < SPIN_CAP; ++spins) {
              __builtin_amdgcn_s_sleep(2);
              e2 = __hip_atomic_load(ep, RLX_AGENT);
            }
            xw = (unsigned)(e2 >> 32);
            if (lane == 0) __hip_atomic_store(ctl + A_OFF / 4 + bid, xw, RLX_AGENT);
          }
          if (lane == 0) *(volatile LAS unsigned*)(L + XWORD) = xw;
        }
      }
    }
  }
  SCHED0();
  typedef unsigned u32x6 __attribute__((ext_vector_type(6)));
  const f32x4 z4 = {0.f, 0.f, 0.f, 0.f};
  const float cexp = 0.08838834764831845f * 1.4426950408889634f;
  float inf = __builtin_inff();
  PIN_S(inf);
  auto cvt2 = [&](float a, float b) {
    typedef float f32x2 __attribute__((ext_vector_type(2)));
    typedef __bf16 bf16x2 __attribute__((ext_vector_type(2)));
    unsigned r = __builtin_bit_cast(unsigned, __builtin_convertvector(f32x2{a, b}, bf16x2));
    PIN_V(r);
    return r;
  };
  u32x4 qf[4];
  f32x4 sc[2][3];
  f32x4 oa[2][2];
  u32x4 pf[2], pl[2], od;
  u32x6 vf[8];
  float mx, tx, sa, sb;
  auto QL = [&](int m, int c) { acc[m][c] = mfma32(wf[1][c], xf[1][m], acc[m][c]); PIN_V(acc[m][c]); SCHED0(); };
  auto QC = [&](int m, int c) { qf[c >> 1][2 * (c & 1)] = cvt2(acc[m][c][0], acc[m][c][1]); qf[c >> 1][2 * (c & 1) + 1] = cvt2(acc[m][c][2], acc[m][c][3]); };
  auto QK = [&](int m, int i) {
    const int s = i / 3, st = i % 3;
    sc[m & 1][st] = mfma32(kf[st][s], __builtin_bit_cast(bf16x8, qf[s]), s ? sc[m & 1][st] : z4);
    PIN_V(sc[m & 1][st]);
    SCHED0();
  };
  auto PV = [&](int m, int i) {
    const int s = i >> 2, h = i & 1, c = 2 * s + h;
    if (m == 0 && !(i & 2)) PIN_V(vf[c]);
    if (i & 2) oa[s & 1][h] = mfma32(__builtin_bit_cast(bf16x8, __builtin_shufflevector(vf[c], vf[c], 2, 3, 4, 5)), __builtin_bit_cast(bf16x8, pl[m & 1]), oa[s & 1][h]);
    else oa[s & 1][h] = mfma32(__builtin_bit_cast(bf16x8, __builtin_shufflevector(vf[c], vf[c], 0, 1, 2, 3)), __builtin_bit_cast(bf16x8, pf[m & 1]), z4);
    PIN_V(oa[s & 1][h]);
    SCHED0();
  };
  const __amdgpu_buffer_rsrc_t ro = __builtin_amdgcn_make_buffer_rsrc((void*)obase, 0, 2 * MS + CS + 2048, 0x00020000);
  auto OC = [&](int s, int h) { od[2 * h] = cvt2(oa[s & 1][h][0], oa[s & 1][h][1]); od[2 * h + 1] = cvt2(oa[s & 1][h][2], oa[s & 1][h][3]); };
  auto OS = [&](int m, int s) { __builtin_amdgcn_raw_buffer_store_b128(od, ro, l16 + (s & 1) * 1024, m * MS + (s >> 1) * CS, 16); };
  auto SM = [&](int m, int p) {
    f32x4 (&x)[3] = sc[m & 1];
    if (p == 0) {
      mx = fmaxf(fmaxf(x[0][0], x[0][1]), x[0][2]); mx = fmaxf(fmaxf(mx, x[0][3]), x[1][0]); mx = fmaxf(fmaxf(mx, x[1][1]), x[1][2]); PIN_V(mx);
    } else if (p == 1) {
      mx = fmaxf(fmaxf(mx, x[1][3]), x[2][0]); mx = fmaxf(fmaxf(mx, x[2][1]), x[2][2]); mx = __builtin_amdgcn_fmed3f(mx, x[2][3], inf);
      tx = mx; PIN_V(mx); PIN_V(tx);
    } else if (p == 2) {
      auto r = __builtin_amdgcn_permlane16_swap(__float_as_uint(mx), __float_as_uint(tx), false, false);
      mx = __builtin_amdgcn_fmed3f(__uint_as_float(r[0]), __uint_as_float(r[1]), inf);
      tx = mx; PIN_V(mx); PIN_V(tx);
    } else if (p == 3) {
      auto r = __builtin_amdgcn_permlane32_swap(__float_as_uint(mx), __float_as_uint(tx), false, false);
      mx = __builtin_amdgcn_fmed3f(__uint_as_float(r[0]), __uint_as_float(r[1]), inf) * cexp;
      PIN_V(mx);
    } else if (p < 10) {
      const int i = 2 * (p - 4);
      float e0_ = __builtin_amdgcn_exp2f(x[i >> 2][i & 3] * cexp - mx), e1_ = __builtin_amdgcn_exp2f(x[i >> 2][(i & 3) + 1] * cexp - mx);
      PIN_V(e0_); PIN_V(e1_);
      x[i >> 2][i & 3] = e0_; x[i >> 2][(i & 3) + 1] = e1_;
    } else if (p == 10) {
      sa = (((x[0][0] + x[0][1]) + x[0][2]) + x[0][3]) + x[1][0]; PIN_V(sa);
    } else if (p == 11) {
      sa = (((sa + x[1][1]) + x[1][2]) + x[1][3]) + x[2][0]; PIN_V(sa);
    } else if (p == 12) {
      sa = ((sa + x[2][1]) + x[2][2]) + x[2][3]; sb = sa; PIN_V(sa); PIN_V(sb);
    } else if (p == 13) {
      auto r = __builtin_amdgcn_permlane16_swap(__float_as_uint(sa), __float_as_uint(sb), false, false);
      sa = __uint_as_float(r[0]) + __uint_as_float(r[1]); sb = sa; PIN_V(sa); PIN_V(sb);
    } else if (p == 14) {
      auto r = __builtin_amdgcn_permlane32_swap(__float_as_uint(sa), __float_as_uint(sb), false, false);
      sa = __builtin_amdgcn_rcpf(__uint_as_float(r[0]) + __uint_as_float(r[1])); PIN_V(sa);
    } else {
      const int i = 2 * (p - 15), j = i - 2;
      if (p < 21) {
        float a = x[i >> 2][i & 3] * sa; PIN_V(a);
        float b = x[i >> 2][(i & 3) + 1] * sa; PIN_V(b);
        x[i >> 2][i & 3] = a; x[i >> 2][(i & 3) + 1] = b;
        SCHED0();
      }
      if (p > 15) {
        const unsigned r = cvt2(x[j >> 2][j & 3], x[j >> 2][(j & 3) + 1]);
        if (j < 8) pf[m & 1][j >> 1] = r; else pl[m & 1][(j >> 1) - 2] = r;
      }
    }
  };
  LAS unsigned char* va[8];
  auto VA = [&](int c) { va[c] = (LAS unsigned char*)(uintptr_t)(va0 ^ (32 * c)); if (c) PIN_V(va[c]); };
  auto VR = [&](int c) {
    const u32x2 t0 = __builtin_bit_cast(u32x2, tr_read(va[c] + (SPARE + 16384 - V12))), t1 = __builtin_bit_cast(u32x2, tr_read(va[c])), t2 = __builtin_bit_cast(u32x2, tr_read(va[c] + 16384));
    vf[c] = u32x6{t0[0], t0[1], t1[0], t1[1], t2[0], t2[1]};
  };
  unsigned xl;
#define S_ SCHED0();
  QL(0, 0); read_k1(2, 0); S_
  QL(0, 1); read_k1(2, 1); S_
  QL(0, 2); read_k1(2, 2); S_
  QL(0, 3); QC(0, 0); S_
  QL(0, 4); QC(0, 1); S_
  QL(0, 5); QC(0, 2); S_
  QL(0, 6); QC(0, 3); S_
  QL(0, 7); QC(0, 4); S_
  QK(0, 0); QC(0, 5); S_
  QK(0, 1); read_k1(2, 3); S_
  QK(0, 2); QC(0, 6); S_
  QK(0, 3); QC(0, 7); S_
  QK(0, 4); xl = *(volatile LAS unsigned*)(L + XWORD); S_
  QK(0, 5); pl[0][0] = 0; pl[0][1] = 0; S_
  QK(0, 6); pl[1][0] = 0; pl[1][1] = 0; S_
  QK(0, 7); VA(0); S_
  QK(0, 8); VA(1); VA(2); S_
  QK(0, 9); VA(3); VA(4); S_
  QK(0, 10); VA(5); VA(6); S_
  QK(0, 11); VA(7); S_
  QL(1, 0); S_
  QL(1, 1); SM(0, 0); S_
  QL(1, 2); SM(0, 1); S_
  QL(1, 3); SM(0, 2); S_
  QL(1, 4); SM(0, 3); S_
  QL(1, 5); SM(0, 4); S_
  QL(1, 6); SM(0, 5); S_
  QL(1, 7); SM(0, 6); S_
  QL(2, 0); SM(0, 7); S_
  QL(2, 1); SM(0, 8); S_
  QL(2, 2); SM(0, 9); S_
  QL(2, 3); SM(0, 10); S_
  QL(2, 4); SM(0, 11); S_
  QL(2, 5); SM(0, 12); S_
  QL(2, 6); SM(0, 13); QC(1, 0); S_
  QL(2, 7); SM(0, 14); QC(1, 1); S_
  BAR();
  S_
  QK(1, 0); VR(0); QC(1, 2); S_
  QK(1, 1); VR(1); QC(1, 3); S_
  QK(1, 2); VR(2); SM(0, 15); S_
  QK(1, 3); VR(3); SM(0, 16); S_
  QK(1, 4); QC(1, 4); SM(0, 17); S_
  QK(1, 5); QC(1, 5); SM(0, 18); S_
  QK(1, 6); QC(1, 6); SM(0, 19); S_
  QK(1, 7); QC(1, 7); SM(0, 20); S_
  QK(1, 8); VR(4); SM(0, 21); S_
  PV(0, 0); VR(5); S_
  PV(0, 1); VR(6); S_
  QK(1, 9); VR(7); S_
  PV(0, 2); S_
  PV(0, 3); S_
  QK(1, 10); QC(2, 0); S_
  QK(1, 11); OC(0, 0); S_
  PV(0, 4); OC(0, 1); OS(0, 0); S_
  PV(0, 5); QC(2, 1); S_
  PV(0, 6); SM(1, 0); S_
  PV(0, 7); SM(1, 1); S_
  PV(0, 8); SM(1, 2); S_
  PV(0, 9); SM(1, 3); OC(1, 0); S_
  PV(0, 10); OC(1, 1); OS(0, 1); S_
  PV(0, 11); SM(1, 4); S_
  PV(0, 12); SM(1, 5); S_
  PV(0, 13); OC(2, 0); QC(2, 2); S_
  PV(0, 14); OC(2, 1); OS(0, 2); S_
  PV(0, 15); SM(1, 6); S_
  QK(2, 0); SM(1, 7); S_
  QK(2, 1); OC(3, 0); QC(2, 3); S_
  QK(2, 2); OC(3, 1); OS(0, 3); S_
  QK(2, 3); SM(1, 8); QC(2, 4); S_
  QK(2, 4); SM(1, 9); QC(2, 5); S_
  QK(2, 5); SM(1, 10); S_
  QK(2, 6); SM(1, 11); QC(2, 6); S_
  QK(2, 7); SM(1, 12); QC(2, 7); S_
  QK(2, 8); SM(1, 13); S_
  QK(2, 9); SM(1, 14); S_
  QK(2, 10); SM(1, 15); SM(1, 16); S_
  QK(2, 11); SM(1, 17); SM(1, 18); SM(1, 19); S_
  PV(1, 0); SM(1, 20); S_
  PV(1, 1); SM(1, 21); SM(2, 0); S_
  PV(1, 2); SM(2, 1); S_
  PV(1, 3); SM(2, 2); S_
  PV(1, 4); SM(2, 3); S_
  PV(1, 5); OC(0, 0); SM(2, 4); S_
  PV(1, 6); OC(0, 1); OS(1, 0); SM(2, 5); S_
  PV(1, 7); SM(2, 6); S_
  PV(1, 8); SM(2, 7); S_
  PV(1, 9); OC(1, 0); SM(2, 8); S_
  PV(1, 10); OC(1, 1); OS(1, 1); SM(2, 9); S_
  PV(1, 11); SM(2, 10); SM(2, 11); S_
  PV(1, 12); SM(2, 12); S_
  PV(1, 13); SM(2, 13); OC(2, 0); S_
  PV(1, 14); SM(2, 14); OC(2, 1); OS(1, 2); S_
  PV(1, 15); SM(2, 15); SM(2, 16); SM(2, 17); SM(2, 18); SM(2, 19); S_
  PV(2, 0); SM(2, 20); S_
  PV(2, 1); SM(2, 21); OC(3, 0); S_
  PV(2, 2); OC(3, 1); OS(1, 3); S_
  PV(2, 3); S_
  PV(2, 4); S_
  PV(2, 5); OC(0, 0); S_
  PV(2, 6); OC(0, 1); OS(2, 0); S_
  PV(2, 7); S_
  PV(2, 8); S_
  PV(2, 9); OC(1, 0); S_
  PV(2, 10); OC(1, 1); OS(2, 1); S_
  PV(2, 11); S_
  PV(2, 12); S_
  PV(2, 13); OC(2, 0); S_
  PV(2, 14); OC(2, 1); OS(2, 2); S_
  PV(2, 15); S_
  OC(3, 0); OC(3, 1); OS(2, 3); S_
  const unsigned X = __builtin_amdgcn_readfirstlane(xl);
  asm volatile("s_waitcnt vmcnt(0)\n\ts_mov_b64 exec, 1\n\tglobal_store_dword %0, %1, %2 sc1\n\ts_mov_b64 exec, -1"
               : : "v"(vzero), "v"(xl), "s"(f1) : "memory");
  if (!p2) {
    if (leader) {
#pragma unroll 1
      for (unsigned spins = 0; spins < SPIN_CAP; ++spins) {
        BUMP(vzero, vone);
        const u32x4 la = __builtin_amdgcn_raw_buffer_load_b128(rc, A_OFF + lane * 16, 0, 16);
        if (__all(((la[0] == X) | (lane == LEADER / 4)) & (la[1] == X) & (la[2] == X) & (la[3] == X))) break;
        __builtin_amdgcn_s_sleep(8);
      }
    }
    return;
  }
  WAIT_L0();
  BAR();

  const int wm = w >> 1, wn = w & 1;
  f32x4 ac2[4][4];
#pragma unroll
  for (int m = 0; m < 4; ++m)
#pragma unroll
    for (int c = 0; c < 4; ++c) { ac2[m][c] = f32x4{0.f, 0.f, 0.f, 0.f}; PIN_V(ac2[m][c]); }
  const int ooff = O_RING + wm * 8192 + lane * 16;
  const int w2o = 2048 * (g >> 1) + 1024 * wn + 64 * (4 * (g & 1) + q4) + 16 * (2 * (g & 1) + (p4 >> 1)) + 8 * (p4 & 1);
  bf16x8 of[2][4], wg[2][4];
  LAS unsigned char *aO = L + ooff, *aW0 = L + (W_RING & 15) * 16384 + w2o, *aW1 = L + (W_RING & 15) * 16384 + (w2o ^ 32);
  PIN_V(aO); PIN_V(aW0); PIN_V(aW1);
  auto read_o = [&](int ks, bf16x8 (&od)[4]) {
#pragma unroll
    for (int m = 0; m < 4; ++m) od[m] = *(LAS bf16x8*)(aO + 1024 * ks + m * 2048);
  };
  auto read_w = [&](int ks, bf16x8 (&wd)[4]) {
#pragma unroll
    for (int c = 0; c < 4; ++c) {
      LAS unsigned char* bp = ((c & 1) ? aW1 : aW0) + 8192 * ks + 512 * (c >> 1);
      wd[c] = cat8(tr_read(bp), tr_read(bp + 4096));
    }
  };
  auto read2 = [&](int ks, bf16x8 (&od)[4], bf16x8 (&wd)[4]) { read_o(ks, od); read_w(ks, wd); };
  auto mma2 = [&](bf16x8 (&od)[4], bf16x8 (&wd)[4]) {
#pragma unroll
    for (int c = 0; c < 4; ++c)
#pragma unroll
      for (int m = 0; m < 4; ++m) ac2[m][c] = mfma32(wd[c], od[m], ac2[m][c]);
  };
  const int tile = br * 30 + i2;
  unsigned char* pbase = P.ws + PART_OFF + (size_t)tile * (4 * 4 * 8 * 1024) + w * 8192;
  GAS unsigned* f2 = ctl + (F2_OFF + (tile * 4 + w) * 128) / 4;
  unsigned char* obuf = (unsigned char*)P.out;
  int ooffs = ((mt * 128 + wm * 64 + kq * 16 + r16) * 1280 + br * 640 + nt * 128 + wn * 64 + (g & 1) * 16 + (g >> 1) * 8) * 2;
  unsigned fa = ((kq + 1 + (lane & 3) % 3) & 3) * 4, fs = kq * 4;
  int l16b = l16 + 4096;
  int so[4];
#pragma unroll
  for (int j = 0; j < 4; ++j) { so[j] = ((kq - j) & 3) * 32768; PIN_S(so[j]); }
  PIN_S(pbase); PIN_S(f2); PIN_S(obuf); PIN_V(ooffs); PIN_V(fa); PIN_V(fs); PIN_V(l16b);
  read_w(0, wg[0]);
  SCHED0();
  BAR();
  read_o(0, of[0]);
  __builtin_amdgcn_s_waitcnt(0xC07F);
  {
    int os = 0;
    unsigned tab = W_RING;
#pragma unroll 1
    for (int j = 0; j < 15; ++j) {
      SCHED0();
      read2(1, of[1], wg[1]);
      mma2(of[0], wg[0]);
      const int dO = (os == 2) ? -32768 : 16384;
      os = (os == 2) ? 0 : os + 1;
      const int dW = ((int)((tab >> 4) & 15) - (int)(tab & 15)) * 16384;
      tab = (tab >> 4) | ((tab & 15) << 24);
      aO += dO; aW0 += dW; aW1 += dW;
#pragma unroll
      for (int i = 0; i < 12; ++i) {
        __builtin_amdgcn_sched_group_barrier(0x008, 1, 0);
        __builtin_amdgcn_sched_group_barrier(0x100, 1, 0);
        __builtin_amdgcn_sched_group_barrier(0x004, 1, 0);
      }
#pragma unroll
      for (int i = 0; i < 3; ++i) {
        __builtin_amdgcn_sched_group_barrier(0x008, 1, 0);
        __builtin_amdgcn_sched_group_barrier(0x002, 1, 0);
      }
      __builtin_amdgcn_sched_group_barrier(0x008, 1, 0);
      SCHED0();
      PIN_V(aO); PIN_V(aW0); PIN_V(aW1);
      __builtin_amdgcn_s_waitcnt(0xC07F);
      BAR();
      SCHED0();
      read2(0, of[0], wg[0]);
      mma2(of[1], wg[1]);
      interleave(12, 4);
    }
  }
  SCHED0();
  read2(1, of[1], wg[1]);
  mma2(of[0], wg[0]);
  interleave(12, 4);
  SCHED0();
  const __amdgpu_buffer_rsrc_t rs = __builtin_amdgcn_make_buffer_rsrc((void*)pbase, 0, 4 * 4 * 8 * 1024, 0x00020000);
  auto LM = [&](int j, int c) { ac2[j][c] = mfma32(wg[1][c], of[1][j], ac2[j][c]); PIN_V(ac2[j][c]); SCHED0(); };
  auto PC = [&](int j, int c) { od[2 * (c & 1)] = cvt2(ac2[j][c][0], ac2[j][c][1]); od[2 * (c & 1) + 1] = cvt2(ac2[j][c][2], ac2[j][c][3]); };
  auto PSt = [&](int j, int c) { __builtin_amdgcn_raw_buffer_store_b128(od, rs, (j < 2 ? l16 : l16b) + ((j * 2 + c) & 3) * 1024, so[0], 16); };
  LM(1, 0); S_
  LM(1, 1); S_
  LM(1, 2); S_
  LM(1, 3); PC(1, 0); S_
  LM(2, 0); PC(1, 1); PSt(1, 0); S_
  LM(2, 1); PC(1, 2); S_
  LM(2, 2); PC(1, 3); PSt(1, 1); S_
  LM(2, 3); PC(2, 0); S_
  LM(3, 0); PC(2, 1); PSt(2, 0); S_
  LM(3, 1); PC(2, 2); S_
  LM(3, 2); PC(2, 3); PSt(2, 1); S_
  LM(3, 3); PC(3, 0); S_
  LM(0, 0); PC(3, 1); PSt(3, 0); S_
  LM(0, 1); PC(3, 2); S_
  LM(0, 2); PC(3, 3); PSt(3, 1); S_
  LM(0, 3); S_
  unsigned fv;
  asm volatile("global_load_dword %0, %1, %4 sc1\n\ts_waitcnt vmcnt(1)\n\ts_mov_b64 exec, 1\n\tglobal_store_dword %2, %3, %4 sc1\n\ts_mov_b64 exec, -1\n\ts_waitcnt vmcnt(1)"
               : "=&v"(fv) : "v"(fa), "v"(fs), "v"(xl), "s"(f2) : "memory");
#pragma unroll 1
  for (unsigned spins = 0; !__all(fv == X) && spins < SPIN_CAP; ++spins)
    asm volatile("global_load_dword %0, %1, %2 sc1\n\ts_waitcnt vmcnt(0)" : "=v"(fv) : "v"(fa), "s"(f2) : "memory");
  u32x4 pr[3][2];
#pragma unroll
  for (int j = 1; j < 4; ++j)
#pragma unroll
    for (int c = 0; c < 2; ++c)
      pr[j - 1][c] = __builtin_amdgcn_raw_buffer_load_b128(rs, (j < 2 ? l16 : l16b) + ((j * 2 + c) & 3) * 1024, so[j], 16);
  u32x2 ov[4];
#pragma unroll
  for (int c = 0; c < 4; ++c) {
    f32x4 s = ac2[0][c];
#pragma unroll
    for (int j = 0; j < 3; ++j) {
      const unsigned lo = pr[j][c >> 1][2 * (c & 1)], hi = pr[j][c >> 1][2 * (c & 1) + 1];
      s += __builtin_bit_cast(f32x4, u32x4{lo << 16, lo & 0xffff0000u, hi << 16, hi & 0xffff0000u});
    }
    ov[c] = __builtin_bit_cast(u32x2, __builtin_convertvector(s, bf16x4));
  }
  {
    const __amdgpu_buffer_rsrc_t rO = __builtin_amdgcn_make_buffer_rsrc((void*)obuf, 0, 768 * 2560, 0x00020000);
#pragma unroll
    for (int c = 0; c < 4; c += 2) {
      auto t0 = __builtin_amdgcn_permlane16_swap(ov[c][0], ov[c + 1][0], false, false);
      auto t1 = __builtin_amdgcn_permlane16_swap(ov[c][1], ov[c + 1][1], false, false);
      __builtin_amdgcn_raw_buffer_store_b128(u32x4{t0[0], t1[0], t0[1], t1[1]}, rO, ooffs + c * 32, 0, 16);
    }
  }
  asm volatile(".Lcode_to:" : : "v"(junk));
}

extern "C" void kernel_launch(void* const* d_in, const int* in_sizes, int n_in, void* d_out, int out_size,
                              void* d_ws, size_t ws_size, hipStream_t stream) {
  Ptrs P;
  for (int i = 0; i < 2; ++i) {
    P.x[i] = (const unsigned short*)d_in[5 * i + 0];
    P.k[i] = (const unsigned short*)d_in[5 * i + 1];
    P.v[i] = (const unsigned short*)d_in[5 * i + 2];
    P.wq[i] = (const unsigned short*)d_in[5 * i + 3];
    P.wo[i] = (const unsigned short*)d_in[5 * i + 4];
  }
  P.out = (unsigned short*)d_out;
  P.ws = (unsigned char*)d_ws + (ws_size >= ((size_t)64 << 20) ? (ws_size - ((size_t)32 << 20)) & ~(size_t)0xfffff : 0);
  (void)hipFuncSetAttribute((const void*)fused, hipFuncAttributeMaxDynamicSharedMemorySize, LDS_BYTES);
  fused<<<256, 512, LDS_BYTES, stream>>>(P);
}
```
